```python
import torch

def _softmax(x, axis=-1):
    return torch.softmax(x, dim=axis)
def _gelu(x):
    return torch.nn.functional.gelu(x, approximate="tanh")
def _silu(x):
    return torch.nn.functional.silu(x)
def _sigmoid(x):
    return torch.sigmoid(x)
def _layernorm(x, axis=-1, eps=1e-5):
    m = x.mean(dim=axis, keepdim=True)
    v = x.var(dim=axis, keepdim=True, unbiased=False)
    return (x - m) / torch.sqrt(v + eps)
def _scatter_add(tgt, idx, src, axis=0):
    return tgt.clone().index_add_(axis, idx, src)
def _segment_sum(src, seg, n):
    out = torch.zeros((n,) + src.shape[1:], dtype=src.dtype)
    return out.index_add_(0, seg, src)
def _pad(x, pw):
    flat = tuple(v for pair in reversed(pw) for v in pair)
    return torch.nn.functional.pad(x, flat)

kernel_name = 'sequential_preLN__gqa_block_cached__mixer_token_mlp__decode'

def setup_inputs():

    _w = (lambda a: (lambda t: t.to(torch.bfloat16) if t.is_floating_point() else t)(torch.as_tensor(a)))
    def _A_setup():
        B, Sq, D = 2, 1, 640
        Hq, Hkv, Sk, Dh = 32, 32, 8192, 192
        assert Hq % Hkv == 0, (Hq, Hkv)
        return {
            "x": _w((lambda *s: torch.randn(*s, dtype=torch.bfloat16) * 0.05)(B, Sq, D)),
            "k_cache": _w((lambda *s: torch.randn(*s, dtype=torch.bfloat16) * 1.6)(B, Hkv, Sk, Dh)),
            "v_cache": _w((lambda *s: torch.randn(*s, dtype=torch.bfloat16) * 0.8)(B, Hkv, Sk, Dh)),
            "wq": _w((lambda *s: torch.randn(*s, dtype=torch.bfloat16) * 1.6)(D, Hq * Dh) * (D ** -0.5)),
            "wo": _w((lambda *s: torch.randn(*s, dtype=torch.bfloat16) * 0.8)(Hq * Dh, D) * ((Hq * Dh) ** -0.5)),
        }
    _A_in = _A_setup()

    def _B_setup():
        B, S, D, F = 2, 1, 640, 1536
        return {
            "x": _w((lambda *s: torch.randn(*s, dtype=torch.bfloat16) * 0.05)(B, S, D)),
            "w1": _w((lambda *s: torch.randn(*s, dtype=torch.bfloat16) * 1.6)(S, F) * (S ** -0.5)),
            "b1": _w((lambda *s: torch.randn(*s, dtype=torch.bfloat16) * 1.6)(F) * 0.02),
            "w2": _w((lambda *s: torch.randn(*s, dtype=torch.bfloat16) * 1.6)(F, S) * (F ** -0.5)),
            "b2": _w((lambda *s: torch.randn(*s, dtype=torch.bfloat16) * 0.8)(S) * 0.02),
        }
    _B_in = _B_setup()

    return {
        "A_x": _A_in["x"],
        "A_k_cache": _A_in["k_cache"],
        "A_v_cache": _A_in["v_cache"],
        "A_wq": _A_in["wq"],
        "A_wo": _A_in["wo"],
        "B_x": _B_in["x"],
        "B_w1": _B_in["w1"],
        "B_b1": _B_in["b1"],
        "B_w2": _B_in["w2"],
        "B_b2": _B_in["b2"],
    }

def reference(A_x, A_k_cache, A_v_cache, A_wq, A_wo, B_x, B_w1, B_b1, B_w2, B_b2):
    def _A_ref(x, k_cache, v_cache, wq, wo):
        B, Sq, D = x.shape
        Hq, Dh = 32, 192
        Hkv = k_cache.shape[1]
        rep = Hq // Hkv
        q = (lambda x, *d: x.permute(*d))((x @ wq).reshape(B, Sq, Hq, Dh), 0, 2, 1, 3)
        k = (lambda x, n, axis: torch.repeat_interleave(x, n, dim=axis))(k_cache, rep, axis=1)
        v = (lambda x, n, axis: torch.repeat_interleave(x, n, dim=axis))(v_cache, rep, axis=1)
        scores = torch.einsum("bhqd,bhkd->bhqk", q, k) * (Dh ** -0.5)
        p = _softmax(scores, axis=-1)
        o = torch.einsum("bhqk,bhkd->bhqd", p, v)
        return (lambda x, *d: x.permute(*d))(o, 0, 2, 1, 3).reshape(B, Sq, Hq * Dh) @ wo

    def _B_ref(x, w1, b1, w2, b2):
        B, S, D = x.shape
        h = _layernorm(x, axis=-1)
        u = torch.einsum("bsd,sf->bdf", h, w1) + b1
        u = _gelu(u)
        y = torch.einsum("bdf,fs->bds", u, w2) + b2
        return (lambda x, *d: x.permute(*d))(y, 0, 2, 1)

    x = A_x
    h = x + _A_ref(_layernorm(x, axis=-1), A_k_cache, A_v_cache, A_wq, A_wo)
    h2 = h + _B_ref(_layernorm(h, axis=-1), B_w1, B_b1, B_w2, B_b2)
    return h2
```

```cpp
#include <hip/hip_runtime.h>
#include <hip/hip_bf16.h>
#include <stdint.h>
#include <stdio.h>

constexpr int B = 2, D = 640, H = 32, SK = 8192, DH = 192, F = 1536;
constexpr int NSPLIT = 8, KPS = SK / NSPLIT;

__device__ __forceinline__ float bf2f(unsigned short v) { return __uint_as_float((unsigned)v << 16); }
__device__ __forceinline__ float rbf(float f) { return __bfloat162float(__float2bfloat16(f)); }

__device__ float block_sum(float v, float* red) {
  for (int o = 32; o > 0; o >>= 1) v += __shfl_xor(v, o);
  int w = threadIdx.x >> 6, nw = blockDim.x >> 6;
  __syncthreads();
  if ((threadIdx.x & 63) == 0) red[w] = v;
  __syncthreads();
  float s = 0.f;
  for (int i = 0; i < nw; ++i) s += red[i];
  return s;
}

__global__ void k_q(const unsigned short* x, const unsigned short* wq, float* q) {
  __shared__ float xn[B][D];
  __shared__ float red[8];
  __shared__ float part[4][B][64];
  for (int b = 0; b < B; ++b) {
    float s = 0.f;
    for (int i = threadIdx.x; i < D; i += 256) s += bf2f(x[b * D + i]);
    float mean = block_sum(s, red) / D;
    float v = 0.f;
    for (int i = threadIdx.x; i < D; i += 256) { float d = bf2f(x[b * D + i]) - mean; v += d * d; }
    float var = block_sum(v, red) / D;
    float rs = 1.f / sqrtf(var + 1e-5f);
    for (int i = threadIdx.x; i < D; i += 256) xn[b][i] = rbf((bf2f(x[b * D + i]) - mean) * rs);
  }
  __syncthreads();
  int c = threadIdx.x & 63, g = threadIdx.x >> 6;
  int n = blockIdx.x * 64 + c;
  float a0 = 0.f, a1 = 0.f;
  for (int k = g; k < D; k += 4) { float w = bf2f(wq[(size_t)k * (H * DH) + n]); a0 += xn[0][k] * w; a1 += xn[1][k] * w; }
  part[g][0][c] = a0; part[g][1][c] = a1;
  __syncthreads();
  if (threadIdx.x < 128) {
    int b = threadIdx.x >> 6;
    float s = part[0][b][c] + part[1][b][c] + part[2][b][c] + part[3][b][c];
    q[b * H * DH + n] = rbf(s);
  }
}

__global__ void k_attn(const unsigned short* K, const unsigned short* V, const float* q, float* part) {
  __shared__ float sm[8][DH + 2];
  int s = blockIdx.x, bh = blockIdx.y;
  int lane = threadIdx.x & 63, w = threadIdx.x >> 6, hf = lane >> 5, c = lane & 31;
  float qv[6];
  for (int j = 0; j < 6; ++j) qv[j] = q[bh * DH + c * 6 + j];
  const unsigned short* kb = K + ((size_t)bh * SK + (size_t)s * KPS) * DH;
  const unsigned short* vb = V + ((size_t)bh * SK + (size_t)s * KPS) * DH;
  float m = -INFINITY, l = 0.f, acc[6] = {0, 0, 0, 0, 0, 0};
  for (int k = w * 2 + hf; k < KPS; k += 8) {
    const unsigned* kp = (const unsigned*)(kb + (size_t)k * DH + c * 6);
    const unsigned* vp = (const unsigned*)(vb + (size_t)k * DH + c * 6);
    unsigned k0 = kp[0], k1 = kp[1], k2 = kp[2], v0 = vp[0], v1 = vp[1], v2 = vp[2];
    float d = qv[0] * __uint_as_float(k0 << 16) + qv[1] * __uint_as_float(k0 & 0xffff0000u) + qv[2] * __uint_as_float(k1 << 16) +
              qv[3] * __uint_as_float(k1 & 0xffff0000u) + qv[4] * __uint_as_float(k2 << 16) + qv[5] * __uint_as_float(k2 & 0xffff0000u);
    for (int o = 16; o > 0; o >>= 1) d += __shfl_xor(d, o);
    float sc = d * 0.07216878364870322f;
    float mn = fmaxf(m, sc), al = __expf(m - mn), p = __expf(sc - mn);
    l = l * al + p; m = mn;
    acc[0] = acc[0] * al + p * __uint_as_float(v0 << 16); acc[1] = acc[1] * al + p * __uint_as_float(v0 & 0xffff0000u);
    acc[2] = acc[2] * al + p * __uint_as_float(v1 << 16); acc[3] = acc[3] * al + p * __uint_as_float(v1 & 0xffff0000u);
    acc[4] = acc[4] * al + p * __uint_as_float(v2 << 16); acc[5] = acc[5] * al + p * __uint_as_float(v2 & 0xffff0000u);
  }
  int r = w * 2 + hf;
  for (int j = 0; j < 6; ++j) sm[r][c * 6 + j] = acc[j];
  if (c == 0) { sm[r][DH] = m; sm[r][DH + 1] = l; }
  __syncthreads();
  if (threadIdx.x < DH) {
    float M = -INFINITY;
    for (int i = 0; i < 8; ++i) M = fmaxf(M, sm[i][DH]);
    float L = 0.f, o = 0.f;
    for (int i = 0; i < 8; ++i) { float e = __expf(sm[i][DH] - M); L += e * sm[i][DH + 1]; o += e * sm[i][threadIdx.x]; }
    float* pp = part + ((size_t)bh * NSPLIT + s) * (DH + 2);
    pp[threadIdx.x] = o;
    if (threadIdx.x == 0) { pp[DH] = M; pp[DH + 1] = L; }
  }
}

__global__ void k_proj(const float* part, const unsigned short* wo, float* y) {
  __shared__ float o[B][DH];
  int h = blockIdx.x;
  if (threadIdx.x < B * DH) {
    int b = threadIdx.x / DH, d = threadIdx.x % DH;
    const float* pp = part + (size_t)(b * H + h) * NSPLIT * (DH + 2);
    float M = -INFINITY;
    for (int s = 0; s < NSPLIT; ++s) M = fmaxf(M, pp[s * (DH + 2) + DH]);
    float L = 0.f, a = 0.f;
    for (int s = 0; s < NSPLIT; ++s) { float e = __expf(pp[s * (DH + 2) + DH] - M); L += e * pp[s * (DH + 2) + DH + 1]; a += e * pp[s * (DH + 2) + d]; }
    o[b][d] = rbf(a / L);
  }
  __syncthreads();
  int n = threadIdx.x;
  float a0 = 0.f, a1 = 0.f;
  for (int d = 0; d < DH; ++d) { float w = bf2f(wo[(size_t)(h * DH + d) * D + n]); a0 += o[0][d] * w; a1 += o[1][d] * w; }
  y[(h * B + 0) * D + n] = a0; y[(h * B + 1) * D + n] = a1;
}

__global__ void k_mlp(const float* y, const unsigned short* x, const unsigned short* w1, const unsigned short* b1, const unsigned short* w2,
                      const unsigned short* b2, __hip_bfloat16* out) {
  __shared__ float hs[D];
  __shared__ float hn[D];
  __shared__ float red[8];
  int b = blockIdx.x / 128, e0 = (blockIdx.x % 128) * 5;
  for (int n = threadIdx.x; n < D; n += 256) {
    float a = 0.f;
    for (int h = 0; h < H; ++h) a += y[(h * B + b) * D + n];
    hs[n] = rbf(bf2f(x[b * D + n]) + rbf(a));
  }
  __syncthreads();
  float s = 0.f;
  for (int i = threadIdx.x; i < D; i += 256) s += hs[i];
  float mean = block_sum(s, red) / D;
  float v = 0.f;
  for (int i = threadIdx.x; i < D; i += 256) { float d = hs[i] - mean; v += d * d; }
  float var = block_sum(v, red) / D;
  float rs = 1.f / sqrtf(var + 1e-5f);
  for (int i = threadIdx.x; i < D; i += 256) hn[i] = (hs[i] - mean) * rs;
  __syncthreads();
  s = 0.f;
  for (int i = threadIdx.x; i < D; i += 256) s += hn[i];
  float mean2 = block_sum(s, red) / D;
  v = 0.f;
  for (int i = threadIdx.x; i < D; i += 256) { float d = hn[i] - mean2; v += d * d; }
  float var2 = block_sum(v, red) / D;
  float rs2 = 1.f / sqrtf(var2 + 1e-5f);
  for (int e = 0; e < 5; ++e) {
    float t = (hn[e0 + e] - mean2) * rs2;
    float a = 0.f;
    for (int f = threadIdx.x; f < F; f += 256) {
      float u = t * bf2f(w1[f]) + bf2f(b1[f]);
      float g = 0.5f * u * (1.f + tanhf(0.7978845608028654f * (u + 0.044715f * u * u * u)));
      a += g * bf2f(w2[f]);
    }
    float tot = block_sum(a, red);
    if (threadIdx.x == 0) out[b * D + e0 + e] = __float2bfloat16(hs[e0 + e] + tot + bf2f(b2[0]));
  }
}

extern "C" void kernel_launch(void* const* d_in, const int* in_sizes, int n_in, void* d_out, int out_size, void* d_ws, size_t ws_size,
                              hipStream_t stream) {
  const unsigned short* x = (const unsigned short*)d_in[0];
  const unsigned short* K = (const unsigned short*)d_in[1];
  const unsigned short* V = (const unsigned short*)d_in[2];
  const unsigned short* wq = (const unsigned short*)d_in[3];
  const unsigned short* wo = (const unsigned short*)d_in[4];
  const unsigned short* w1 = (const unsigned short*)d_in[6];
  const unsigned short* b1 = (const unsigned short*)d_in[7];
  const unsigned short* w2 = (const unsigned short*)d_in[8];
  const unsigned short* b2 = (const unsigned short*)d_in[9];
  fprintf(stderr, "PTRS x %p K %p V %p wq %p wo %p w1 %p out %p ws %p wssz %zu\n", d_in[0], d_in[1], d_in[2], d_in[3], d_in[4], d_in[6], d_out, d_ws, ws_size);
  float* q = (float*)d_ws;
  float* part = q + B * H * DH;
  float* y = part + (size_t)B * H * NSPLIT * (DH + 2);
  k_q<<<H * DH / 64, 256, 0, stream>>>(x, wq, q);
  k_attn<<<dim3(NSPLIT, B * H), 256, 0, stream>>>(K, V, q, part);
  k_proj<<<H, D, 0, stream>>>(part, wo, y);
  k_mlp<<<256, 256, 0, stream>>>(y, x, w1, b1, w2, b2, (__hip_bfloat16*)d_out);
}
```

```cpp
#include <hip/hip_runtime.h>
#include <stdint.h>

#define LOCK 4
#define WO_AUX 2
#define WQ_AUX 2
#define CODE_KB 18
#define THR 3
#define LIGHT 7
#define STAMPS 0

namespace fk {
typedef unsigned short u16;
typedef unsigned long long u64;
typedef unsigned v2u __attribute__((ext_vector_type(2)));
typedef unsigned v3u __attribute__((ext_vector_type(3)));
typedef unsigned v4u __attribute__((ext_vector_type(4)));
typedef int v4i __attribute__((ext_vector_type(4)));
typedef float v4f __attribute__((ext_vector_type(4)));
typedef float v2f __attribute__((ext_vector_type(2)));
typedef short v4s __attribute__((ext_vector_type(4)));
typedef __bf16 bf16x2 __attribute__((ext_vector_type(2)));
typedef __bf16 bf16x8 __attribute__((ext_vector_type(8)));
#define GAS __attribute__((address_space(1)))
#define LAS __attribute__((address_space(3)))
#define RLX_AGENT __ATOMIC_RELAXED, __HIP_MEMORY_SCOPE_AGENT
#define NOUNROLL _Pragma("clang loop unroll(disable)")

constexpr int DM = 640, NH = 32, SK = 8192, DH = 192;
constexpr unsigned ROWB = 384, REGION = SK * ROWB, KVBYTES = 64u * REGION;
constexpr unsigned OOB = 0x40000000u;
constexpr int SC1 = 16;
constexpr unsigned WS_EPOCH = 0;
constexpr unsigned WS_PROBE = 4096;
constexpr unsigned WS_Q = 8192;
constexpr unsigned WS_H = WS_Q + 2 * 32 * 96 * 8;
constexpr unsigned WS_Y = WS_H + 4096;
constexpr unsigned PSTR = 208;
constexpr unsigned WS_P = WS_Y + 32 * 2 * 640 * 8;
constexpr unsigned WS_S = WS_P + 32 * 2 * 8 * PSTR * 8;
constexpr unsigned WS_END = WS_S + 64 * 8 * 8 * 640;
constexpr unsigned WS_STAMP = 8u << 20;
constexpr unsigned SPIN_CAP = 1u << 17;
constexpr float LOG2E = 1.4426950408889634f;

__device__ __forceinline__ float bf2f(unsigned v) { return __uint_as_float(v << 16); }
__device__ __forceinline__ unsigned f2bf(float f) { return (unsigned)__builtin_bit_cast(u16, (__bf16)f); }
__device__ __forceinline__ float rbf(float f) { return bf2f(f2bf(f)); }
__device__ __forceinline__ float lo16(unsigned v) { return __uint_as_float(v << 16); }
__device__ __forceinline__ float hi16(unsigned v) { return __uint_as_float(v & 0xffff0000u); }
__device__ __forceinline__ v2f unpk(unsigned v) { return v2f{lo16(v), hi16(v)}; }
__device__ __forceinline__ v2f pkfma(v2f a, v2f b, v2f c) { return __builtin_elementwise_fma(a, b, c); }
__device__ __forceinline__ float dot2(unsigned a, unsigned b, float c) {
  return __builtin_amdgcn_fdot2_f32_bf16(__builtin_bit_cast(bf16x2, a), __builtin_bit_cast(bf16x2, b), c, false);
}
template <int CTRL> __device__ __forceinline__ float dpp(float x) {
  return __builtin_bit_cast(float, __builtin_amdgcn_mov_dpp(__builtin_bit_cast(int, x), CTRL, 0xf, 0xf, true));
}
constexpr int XOR1 = 0xB1, XOR2 = 0x4E, HMIR = 0x141, MIR = 0x140, ROR4 = 0x124, ROR8 = 0x128;
__device__ __forceinline__ float row_sum(float x) {
  x += dpp<XOR1>(x); x += dpp<XOR2>(x); x += dpp<HMIR>(x); x += dpp<MIR>(x); return x;
}
__device__ __forceinline__ float row_max(float x) {
  x = fmaxf(x, dpp<XOR1>(x)); x = fmaxf(x, dpp<XOR2>(x)); x = fmaxf(x, dpp<HMIR>(x)); x = fmaxf(x, dpp<MIR>(x)); return x;
}
__device__ __forceinline__ float swap16_sum(float a, float b) {
  auto s = __builtin_amdgcn_permlane16_swap(__float_as_uint(a), __float_as_uint(b), false, false);
  return __uint_as_float(s[0]) + __uint_as_float(s[1]);
}
__device__ __forceinline__ float swap32_sum(float a, float b) {
  auto s = __builtin_amdgcn_permlane32_swap(__float_as_uint(a), __float_as_uint(b), false, false);
  return __uint_as_float(s[0]) + __uint_as_float(s[1]);
}
__device__ __forceinline__ float wave_sum(float x) { x = row_sum(x); x = swap16_sum(x, x); return swap32_sum(x, x); }
__device__ __forceinline__ float wave_max(float x) {
  x = row_max(x);
  auto s = __builtin_amdgcn_permlane16_swap(__float_as_uint(x), __float_as_uint(x), false, false);
  x = fmaxf(__uint_as_float(s[0]), __uint_as_float(s[1]));
  auto t = __builtin_amdgcn_permlane32_swap(__float_as_uint(x), __float_as_uint(x), false, false);
  return fmaxf(__uint_as_float(t[0]), __uint_as_float(t[1]));
}
__device__ __forceinline__ float rdl(float x, int l) { return __int_as_float(__builtin_amdgcn_readlane(__float_as_int(x), l)); }
__device__ __forceinline__ v4i mk_rsrc(const void* p, unsigned bytes) {
  const u64 a = (u64)p;
  v4i r; r[0] = (int)(unsigned)a; r[1] = (int)((a >> 32) & 0xffff); r[2] = (int)bytes; r[3] = 0x00020000; return r;
}
__device__ __forceinline__ void st_gran(char* ws, unsigned off, unsigned val, unsigned tag) {
  __hip_atomic_store((GAS u64*)(ws + off), ((u64)tag << 32) | val, RLX_AGENT);
}
__device__ __forceinline__ u64 ld_gran(char* ws, unsigned off) { return __hip_atomic_load((GAS u64*)(ws + off), RLX_AGENT); }
#define BAR() do { asm volatile("s_waitcnt lgkmcnt(0)" ::: "memory"); __builtin_amdgcn_s_barrier(); asm volatile("" ::: "memory"); } while (0)
__device__ __forceinline__ unsigned fcode(unsigned off) { return (((off >> 19) & 1u) << 2) ^ ((off >> 20) & 3u); }
#define STAMP(i) do { } while (0)

struct __attribute__((aligned(16))) Lds {
  float xn[DM][2];
  float qp[8][64];
  unsigned q[64][4];
  float p[8][2][96];
  unsigned sc[2][8][80];
  float ow[8][200];
  float wm[8];
  u16 ob[2][192];
  float t[8];
  float hv[8];
  float red[8][8];
  unsigned flag, flag_pad[3];
  u16 wt[192 * 80];
  char pad[36 * 1024];
};

#define LOADBLK(R, rs, so, so5) asm volatile("s_nop 4\n\t" \
    "buffer_load_dwordx3 %0, %6, %8, %9 offen nt\n\t" \
    "buffer_load_dwordx3 %1, %6, %8, %9 offen offset:768 nt\n\t" \
    "buffer_load_dwordx3 %2, %6, %8, %9 offen offset:1536 nt\n\t" \
    "buffer_load_dwordx3 %3, %6, %8, %9 offen offset:2304 nt\n\t" \
    "buffer_load_dwordx3 %4, %6, %8, %9 offen offset:3072 nt\n\t" \
    "buffer_load_dwordx3 %5, %7, %8, %10 offen offset:3840 nt" \
    : "=&v"(R[0]), "=&v"(R[1]), "=&v"(R[2]), "=&v"(R[3]), "=&v"(R[4]), "=&v"(R[5]) \
    : "v"(voff), "v"(voff5), "s"(rs), "s"(so), "s"(so5) : "memory")
#define WAITV(n, r) asm volatile("s_waitcnt vmcnt(%1)" : "+v"(r) : "i"(n) : "memory")
#define LOCKSTEP(d) do { if (LOCK && ((d) % (LOCK ? LOCK : 1)) == LOCK - 1) __builtin_amdgcn_s_barrier(); } while (0)

__global__ void __launch_bounds__(512, 2)
fused(const u16* __restrict__ x, const char* __restrict__ K, const char* __restrict__ V, const char* __restrict__ wq, const u16* __restrict__ wo,
      const u16* __restrict__ w1, const u16* __restrict__ b1, const u16* __restrict__ w2, const u16* __restrict__ b2, u16* __restrict__ out, char* ws) {
  __shared__ Lds L;
  const int tid = threadIdx.x, lane = tid & 63, w = __builtin_amdgcn_readfirstlane(tid >> 6);
  const int bid = blockIdx.x, j = bid & 7, r = bid >> 3;
  const int hf = lane >> 5, l5 = lane & 31;
  unsigned rnd = (unsigned)(__builtin_amdgcn_s_memtime() >> 2);
  unsigned T;
  asm volatile("s_load_dword %0, %2, 0x0" : "=s"(T), "+s"(rnd) : "s"(ws) : "memory");
  const unsigned xcc = (unsigned)__builtin_amdgcn_s_getreg(20 | (3 << 11)) & 7u;
  const __amdgpu_buffer_rsrc_t rwb = __builtin_amdgcn_make_buffer_rsrc((void*)ws, 0, WS_END, 0x00020000);

  unsigned gap = 0;
  v4u xs4 = {0u, 0u, 0u, 0u}; unsigned xs1 = 0;
  if (w == 0) {
    const int rg = xcc & 3;
    const unsigned off = (unsigned)((rg & 1) * 32 + r) * REGION + (32u + (rnd & 31u)) * 32768u;
    const unsigned pa = fcode(off);
    const char* base = ((rg & 2) ? V : K) + off + ((rnd >> 5) & 31u) * 128u;
    const char* A = base + pa * 4096;
    const char* B = base + (pa ^ 2u) * 4096;
    u64 t1, t2; unsigned va, vb; const unsigned z = 0;
    asm volatile("global_load_dword %2, %4, %5 nt\n\t"
                 "global_load_dword %3, %4, %6 nt\n\t"
                 "s_waitcnt vmcnt(1)\n\ts_memtime %0\n\t"
                 "s_waitcnt vmcnt(0)\n\ts_memtime %1\n\t"
                 "s_waitcnt lgkmcnt(0)" : "=&s"(t1), "=&s"(t2), "=&v"(va), "=&v"(vb) : "v"(z), "s"(A), "s"(B) : "memory");
    gap = min((unsigned)(t2 - t1), 0xffffffu);
    if (lane == 0) L.flag = 0u;
  } else if (w <= 3) {
    u64 pc; asm volatile("s_getpc_b64 %0" : "=s"(pc));
    const char* xp = w < 3 ? (const char*)(x + (w - 1) * DM + lane * 10) : (const char*)(pc & ~255ull) + (r < CODE_KB ? r : 0) * 1024 + lane * 16;
    __builtin_amdgcn_s_sleep(4);
    asm volatile("global_load_dwordx4 %0, %2, off\n\tglobal_load_dword %1, %2, off offset:16" : "=&v"(xs4), "=&v"(xs1) : "v"(xp) : "memory");
  }
  asm volatile("s_waitcnt lgkmcnt(0)\n\ts_add_u32 %0, %0, 0x9E3779B9" : "+s"(T) :: "memory", "scc");
  if (tid == 0) st_gran(ws, WS_PROBE + r * 128 + j * 8, (gap << 8) | xcc, T);
  BAR();
  STAMP(1);

  const int h2 = 4 * j + (r >> 3), c2 = r & 7;
  v4u wqv[5];
  {
    const v4i rq = mk_rsrc(wq, DM * NH * DH * 2);
    const unsigned so = (r < 24 ? 0u : OOB) + (unsigned)(80 * w) * (NH * DH * 2) + (768 * j + 32 * r) * 2;
    const unsigned vq = (lane >> 2) * (NH * DH * 2) + (lane & 3) * 16;
#pragma unroll
    for (int i = 0; i < 5; ++i) { const unsigned si = so + i * 16 * (NH * DH * 2); asm volatile("buffer_load_dwordx4 %0, %1, %2, %3 offen nt" : "=&v"(wqv[i]) : "v"(vq), "s"(rq), "s"(si) : "memory"); }
  }
  asm volatile("s_waitcnt vmcnt(5)" : "+v"(xs4), "+v"(xs1) :: "memory");
  if (w == 1 || w == 2) {
    float e[10];
#pragma unroll
    for (int i = 0; i < 4; ++i) { e[2 * i] = lo16(xs4[i]); e[2 * i + 1] = hi16(xs4[i]); }
    e[8] = lo16(xs1); e[9] = hi16(xs1);
    float s = 0.f;
#pragma unroll
    for (int i = 0; i < 10; ++i) s += e[i];
    const float mean = wave_sum(s) * (1.f / DM);
    float v = 0.f;
#pragma unroll
    for (int i = 0; i < 10; ++i) { const float d = e[i] - mean; v = fmaf(d, d, v); }
    const float var = wave_sum(v) * (1.f / DM);
    const float mb = rbf(mean), den = rbf(sqrtf(rbf(rbf(var) + 1e-5f)));
#pragma unroll
    for (int i = 0; i < 10; ++i) L.xn[lane * 10 + i][w - 1] = rbf(rbf(e[i] - mb) / den);
  }
  BAR();
  v2u pg; v4u wov[4];
  const v4i rw = mk_rsrc(ws, WS_END);
  {
    const v4i ro = mk_rsrc(wo, DM * NH * DH * 2);
    const unsigned so = (unsigned)(h2 * DH * DM + 80 * c2) * 2, sp = WS_PROBE + r * 128, vp = (lane & 7) * 8;
    unsigned vo[4];
#pragma unroll
    for (int i = 0; i < 4; ++i) { const unsigned c = tid + 512 * i, row = (c * 6554u) >> 16, pc = c - row * 10u; vo[i] = c < 1920u ? row * (DM * 2) + pc * 16u : OOB; }
    asm volatile("buffer_load_dwordx2 %0, %5, %10, %11 offen sc1\n\t"
                 "buffer_load_dwordx4 %1, %6, %12, %13 offen nt\n\tbuffer_load_dwordx4 %2, %7, %12, %13 offen nt\n\t"
                 "buffer_load_dwordx4 %3, %8, %12, %13 offen nt\n\tbuffer_load_dwordx4 %4, %9, %12, %13 offen nt"
                 : "=&v"(pg), "=&v"(wov[0]), "=&v"(wov[1]), "=&v"(wov[2]), "=&v"(wov[3])
                 : "v"(vp), "v"(vo[0]), "v"(vo[1]), "v"(vo[2]), "v"(vo[3]), "s"(rw), "s"(sp), "s"(ro), "s"(so) : "memory");
  }
  float s2[4] = {0.f, 0.f, 0.f, 0.f};
  {
    v2f acc[2][4];
#pragma unroll
    for (int c = 0; c < 4; ++c) { acc[0][c] = v2f{0.f, 0.f}; acc[1][c] = v2f{0.f, 0.f}; }
#pragma unroll
    for (int i = 0; i < 5; ++i) {
      const v2f xv = *(const v2f*)L.xn[80 * w + 16 * i + (lane >> 2)];
      WAITV(9 - i, wqv[i]);
#pragma unroll
      for (int c = 0; c < 4; ++c) {
        const v2f a = unpk(wqv[i][c]);
        acc[0][c] = pkfma(a, v2f{xv[0], xv[0]}, acc[0][c]);
        acc[1][c] = pkfma(a, v2f{xv[1], xv[1]}, acc[1][c]);
      }
    }
    float s1[8];
#pragma unroll
    for (int c = 0; c < 8; ++c) s1[c] = swap32_sum(acc[0][c >> 1][c & 1], acc[1][c >> 1][c & 1]);
#pragma unroll
    for (int c = 0; c < 4; ++c) { float t = swap16_sum(s1[c], s1[c + 4]); t += dpp<ROR4>(t); t += dpp<ROR8>(t); s2[c] = t; }
  }
  __builtin_amdgcn_sched_barrier(0);
  if ((lane & 12) == 0) *(v4f*)&L.qp[w][hf * 32 + 8 * (lane & 3) + 4 * ((lane >> 4) & 1)] = v4f{s2[0], s2[1], s2[2], s2[3]};

  unsigned labs;
  {
    WAITV(4, pg);
    NOUNROLL
    for (unsigned spin = 0; !__all(pg[1] == T) && spin < SPIN_CAP; ++spin) {
      __builtin_amdgcn_s_sleep(1);
      const unsigned sp = WS_PROBE + r * 128, vp = (lane & 7) * 8;
      asm volatile("buffer_load_dwordx2 %0, %1, %2, %3 offen sc1\n\ts_waitcnt vmcnt(0)" : "=&v"(pg) : "v"(vp), "s"(rw), "s"(sp) : "memory");
    }
    const int gx = (int)pg[0] & 7;
    int msk = 1 << gx;
    msk |= __builtin_amdgcn_mov_dpp(msk, XOR1, 0xf, 0xf, true); msk |= __builtin_amdgcn_mov_dpp(msk, XOR2, 0xf, 0xf, true); msk |= __builtin_amdgcn_mov_dpp(msk, HMIR, 0xf, 0xf, true);
    const unsigned gv = (unsigned)__builtin_amdgcn_ds_permute(gx * 4, (int)pg[0]) >> 8;
    const unsigned go = (unsigned)__builtin_amdgcn_mov_dpp((int)gv, 0x104, 0xf, 0xf, true);
    const unsigned far = (unsigned)__builtin_amdgcn_ballot_w64(go > gv) & 15u;
    labs = (unsigned)j * 0x01010101u;
    if (__builtin_amdgcn_readfirstlane(msk) == 0xff) {
      const unsigned lab = (((xcc >> 1) ^ (unsigned)(r >> 1)) & 1u) | ((xcc >> 2) << 1) | (((xcc ^ (unsigned)r) & 1u) << 2);
      labs = (lab * 0x01010101u) ^ 0x04000400u ^ ((far & 1u) << 1) ^ ((far & 2u) << 8) ^ ((far & 4u) << 15) ^ ((far & 8u) << 22);
    }
  }
  STAMP(3);
  const int b3 = j >> 2, n0 = ((j & 3) * 32 + r) * 5;
  const v4i rk = mk_rsrc(K, KVBYTES), rv = mk_rsrc(V, KVBYTES);
  const unsigned voff = lane * 12, voff5 = hf ? OOB : voff;
  v3u ring[4][6];
  unsigned n11 = 0;
  auto issue = [&](int d, unsigned rev) {
    const unsigned ph = (rev * 11u) >> 5;
    const unsigned per = ((rev - 3u * ph) * 4u + (unsigned)d) * 8u + (unsigned)w;
    const unsigned rbase = ((ph & 1u) * 32u + (unsigned)r) * REGION;
    const unsigned k = per * 8u + (((labs >> ((8u * ph) & 31u)) & 7u) ^ fcode(rbase + per * 32768u));
    const unsigned r0 = ((32u * k + 1u) * 43691u) >> 17;
    const unsigned r1 = ((32u * k + 33u) * 43691u) >> 17;
    const unsigned so = rev < 12u ? rbase + r0 * ROWB : OOB;
    const bool e = (r1 - r0) == 11u;
    const unsigned so5 = e ? so : OOB;
    n11 = (n11 & ~(1u << d)) | (e ? (1u << d) : 0u);
    const v4i rs = rev >= 6u ? rv : rk;
    LOADBLK(ring[d], rs, so, so5);
  };
  unsigned voc = OOB;
  if (tid < 194) voc = WS_P + ((r * 2 + (tid >= 97)) * 8 + ((labs >> (tid >= 97 ? 24 : 16)) & 7u)) * (PSTR * 8) + (tid >= 97 ? tid - 97 : tid) * 16;
  else if (tid >= 256 && tid < 336) voc = WS_Y + ((h2 * 2 + (tid >= 296)) * DM + 80 * c2) * 8 + (tid >= 296 ? tid - 296 : tid - 256) * 16;
  else if (tid == 448) voc = WS_H + (j * 32 + r) * 16;
  v4u old;
  asm volatile("buffer_load_dwordx4 %0, %1, %2, 0 offen sc1" : "=&v"(old) : "v"(voc), "s"(rw) : "memory");
  issue(0, 0u);
  BAR();
  if (r < 24 && tid < 32) {
    float a = 0.f, bq = 0.f;
#pragma unroll
    for (int i = 0; i < 8; ++i) { const v2f t = *(const v2f*)&L.qp[i][2 * tid]; a += t[0]; bq += t[1]; }
    const int b = tid >> 4, col = 768 * j + 32 * r + 2 * (tid & 15);
    st_gran(ws, WS_Q + (b * (NH * 96) + (col >> 1)) * 8, f2bf(a) | (f2bf(bq) << 16), T);
  }
  STAMP(2);

  {
    volatile LAS unsigned* flag = (volatile LAS unsigned*)&L.flag;
    bool have = w != 0;
    const unsigned qoff = WS_Q + ((hf * NH + r) * 96 + 3 * l5) * 8;
    auto poll = [&]() {
      const u64 a = ld_gran(ws, qoff), b = ld_gran(ws, qoff + 8), c = ld_gran(ws, qoff + 16);
      have = __all((unsigned)(a >> 32) == T && (unsigned)(b >> 32) == T && (unsigned)(c >> 32) == T);
      if (have) { L.q[lane][0] = (unsigned)a; L.q[lane][1] = (unsigned)b; L.q[lane][2] = (unsigned)c; *flag = 1u; }
    };
#pragma unroll
    for (int d = 1; d < 4; ++d) {
      if (!have) poll();
      else if (__builtin_amdgcn_readfirstlane(*flag) == 0u) asm volatile("s_waitcnt vmcnt(%0)" :: "i"(THR) : "memory");
      issue(d, 0u);
    }
    NOUNROLL
    for (unsigned spin = 0; !have && spin < SPIN_CAP; ++spin) { poll(); if (!have) __builtin_amdgcn_s_sleep(1); }
  }
  asm volatile("s_waitcnt vmcnt(24)" : "+v"(wov[0]), "+v"(wov[1]), "+v"(wov[2]), "+v"(wov[3]), "+v"(old) :: "memory");
#pragma unroll
  for (int i = 0; i < 4; ++i) if (i < 3 || tid < 384) *(v4u*)&L.wt[(tid + 512 * i) * 8] = wov[i];
  if (voc != OOB && (old[1] == T || old[3] == T)) __builtin_amdgcn_raw_buffer_store_b128(v4u{0u, ~T, 0u, ~T}, rwb, voc, 0, SC1);
  BAR();
  STAMP(4);

  const bool slot_ok = (l5 & 15) < 12;
  const int slot = (l5 >> 4) * 12 + (l5 & 15);
  const unsigned svoff = lane < 40 ? lane * 16u : OOB;
  auto sbase = [&](unsigned ph) { return WS_S + ((((ph & 1u) * 32u + (unsigned)r) * 8u + ((labs >> (8u * ph)) & 7u)) * 8u + (unsigned)w) * 640u; };
  const unsigned cross = labs ^ (labs >> 16);
  v4u sg = {0u, 0u, 0u, 0u};
#define SLOAD(so) asm volatile("s_nop 4\n\tbuffer_load_dwordx4 %0, %1, %2, %3 offen sc1" : "+v"(sg) : "v"(svoff), "s"(rw), "s"(so) : "memory")

  NOUNROLL
  for (unsigned rev = 0; rev < 6; ++rev) {
    const unsigned ph = rev >= 3u;
    if (rev == 5u && (cross & 0xffu)) { const unsigned so = sbase(2u); SLOAD(so); }
    const v3u qv = *(const v3u*)L.q[ph * 32 + l5];
    float col[2] = {0.f, 0.f};
#pragma unroll
    for (int d = 0; d < 4; ++d) {
#pragma unroll
      for (int u = 0; u < 6; ++u) {
        WAITV(23 - u, ring[d][u]);
        float s = dot2(ring[d][u][0], qv[0], 0.f);
        s = dot2(ring[d][u][1], qv[1], s);
        s = dot2(ring[d][u][2], qv[2], s);
        s = row_sum(s);
        if (u == 5) s = (hf == 0 && ((n11 >> d) & 1u)) ? s : -INFINITY;
        col[d >> 1] = ((lane & 15) == (d & 1) * 6 + u) ? s : col[d >> 1];
      }
      asm volatile("" : "+v"(col[d >> 1]));
      issue(d, rev + 1u);
      LOCKSTEP(d);
    }
    const float sc = rbf(swap16_sum(col[0], col[1])) * 0.07216878364870322f;
    const unsigned sb = f2bf(sc);
    auto sw = __builtin_amdgcn_permlane32_swap(sb, sb, false, false);
    if (slot_ok && hf == 0) L.sc[ph][w][(rev - 3u * ph) * 24u + slot] = sw[0] | (sw[1] << 16);
    if (rev - 3u * ph == 2u && ((cross >> (8u * ph)) & 0xffu)) {
      asm volatile("s_waitcnt lgkmcnt(0)" ::: "memory");
      const v2u e = *(const v2u*)&L.sc[ph][w][2 * (lane < 40 ? lane : 0)];
      const v4u g = {e[0], T, e[1], T};
      const unsigned so = sbase(ph);
      asm volatile("s_nop 4\n\tbuffer_store_dwordx4 %0, %1, %2, %3 offen sc1 nt\n\ts_nop 1" :: "v"(g), "v"(svoff), "s"(rw), "s"(so) : "memory");
    }
  }
  STAMP(5);

  unsigned mw1[3], mb1[3], mw2[3], xres0, xres1, xres2, b2v;
  NOUNROLL
  for (unsigned b = 0; b < 2; ++b) {
    if ((cross >> (8u * b)) & 0xffu) {
      WAITV(24, sg);
      const unsigned so = sbase(2u + b);
      NOUNROLL
      for (unsigned spin = 0; !__all(lane >= 40 || (sg[1] == T && sg[3] == T)) && spin < SPIN_CAP; ++spin) {
        __builtin_amdgcn_s_sleep(2);
        SLOAD(so);
        WAITV(0, sg);
      }
      if (lane < 40) *(v2u*)&L.sc[b][w][2 * lane] = v2u{sg[0], sg[2]};
    }
    asm volatile("s_waitcnt lgkmcnt(0)" ::: "memory");
    const unsigned sh = hf ? 0u : 16u;
    const float ninf = -INFINITY;
    const unsigned* sp = &L.sc[b][w][slot];
    const float s0 = slot_ok ? hi16(sp[0] << sh) : ninf, s1 = slot_ok ? hi16(sp[24] << sh) : ninf, s2v = slot_ok ? hi16(sp[48] << sh) : ninf;
    const float mw = wave_max(fmaxf(fmaxf(s0, s1), s2v));
    if (lane == 0) L.wm[w] = mw;
    BAR();
    const v4f ma = *(const v4f*)&L.wm[0], mb = *(const v4f*)&L.wm[4];
    const float M = fmaxf(fmaxf(fmaxf(ma[0], ma[1]), fmaxf(ma[2], ma[3])), fmaxf(fmaxf(mb[0], mb[1]), fmaxf(mb[2], mb[3])));
    const float p0 = __builtin_amdgcn_exp2f((s0 - M) * LOG2E), p1 = __builtin_amdgcn_exp2f((s1 - M) * LOG2E), p2 = __builtin_amdgcn_exp2f((s2v - M) * LOG2E);
    if (slot_ok) { const int q = l5 & 15; float* pp = &L.p[w][hf][(l5 >> 4) * 16 + (q >= 6 ? q + 2 : q)]; pp[0] = p0; pp[32] = p1; pp[64] = p2; }
    const float ls = wave_sum(p0 + p1 + p2);
    v2f acc[3] = {v2f{0.f, 0.f}, v2f{0.f, 0.f}, v2f{0.f, 0.f}};
    asm volatile("s_waitcnt lgkmcnt(0)" ::: "memory");
    NOUNROLL
    for (unsigned t = 0; t < 3; ++t) {
      if (t == 2u && b == 0u && (cross & 0xff00u)) { const unsigned so = sbase(3u); SLOAD(so); }
      float pr[4][6];
#pragma unroll
      for (int d = 0; d < 4; ++d) {
        const v4f a = *(const v4f*)&L.p[w][hf][t * 32 + d * 8]; const v2f c = *(const v2f*)&L.p[w][hf][t * 32 + d * 8 + 4];
        pr[d][0] = a[0]; pr[d][1] = a[1]; pr[d][2] = a[2]; pr[d][3] = a[3]; pr[d][4] = c[0]; pr[d][5] = c[1];
      }
#pragma unroll
      for (int d = 0; d < 4; ++d) {
#pragma unroll
        for (int u = 0; u < 6; ++u) {
          WAITV(23 - u, ring[d][u]);
#pragma unroll
          for (int i = 0; i < 3; ++i) acc[i] = pkfma(unpk(ring[d][u][i]), v2f{pr[d][u], pr[d][u]}, acc[i]);
        }
        asm volatile("" : "+v"(acc[0]), "+v"(acc[1]), "+v"(acc[2]));
        issue(d, 7u + 3u * b + t);
        if (d < 3 || t < 2u) LOCKSTEP(d);
      }
    }
    STAMP(6);
#pragma unroll
    for (int i = 0; i < 3; ++i) L.ow[w][6 * l5 + 2 * i + hf] = swap32_sum(acc[i][0], acc[i][1]);
    if (lane == 0) { L.ow[w][192] = 0.f; L.ow[w][193] = ls; }
    BAR();
    if (tid < 97) {
      v2f v = *(const v2f*)&L.ow[0][2 * tid];
#pragma unroll
      for (int i = 1; i < 8; ++i) v += *(const v2f*)&L.ow[i][2 * tid];
      if (tid == 96) v[0] = M;
      __builtin_amdgcn_raw_buffer_store_b128(v4u{__float_as_uint(v[0]), T, __float_as_uint(v[1]), T}, rwb, tid * 16,
                                             WS_P + ((r * 2 + b) * 8 + ((labs >> (16 + 8 * b)) & 7u)) * (PSTR * 8), SC1);
    }
  }
  STAMP(7);
  WAITV(0, sg);
#pragma unroll
  for (int d = 0; d < 4; ++d)
    asm volatile("" :: "v"(ring[d][0]), "v"(ring[d][1]), "v"(ring[d][2]), "v"(ring[d][3]), "v"(ring[d][4]), "v"(ring[d][5]));
#pragma unroll
  for (int i = 0; i < 3; ++i) { mw1[i] = w1[tid + 512 * i]; mb1[i] = b1[tid + 512 * i]; mw2[i] = w2[tid + 512 * i]; }
  xres0 = x[b3 * DM + n0 + hf]; xres1 = x[b3 * DM + n0 + 2 + hf]; xres2 = x[b3 * DM + n0 + 4];
  b2v = b2[0];

  v4s wof[6][2];
  {
    const int wc = w < 5 ? w : 0;
    const LAS u16* bp = (const LAS u16*)L.wt + (8 * (lane >> 4) + ((lane & 15) >> 2)) * 80 + 16 * wc + 4 * (lane & 3);
#pragma unroll
    for (int ks = 0; ks < 6; ++ks)
#pragma unroll
      for (int t = 0; t < 2; ++t) wof[ks][t] = __builtin_amdgcn_ds_read_tr16_b64_v4i16((LAS v4s*)(bp + (32 * ks + 4 * t) * 80));
  }
  if (w >= 2 && w < 6) {
    const int b = w < 4, d2 = (w & 1) * 48 + lane;
    const unsigned pb = WS_P + (unsigned)((h2 * 2 + b) * 8) * PSTR * 8;
    const unsigned vo = lane < 48 ? d2 * 16 : 0;
    v4u g[8], ml;
    NOUNROLL
    for (unsigned spin = 0; spin < SPIN_CAP; ++spin) {
      asm volatile("" ::: "memory");
      ml = __builtin_amdgcn_raw_buffer_load_b128(rwb, (lane & 7) * (PSTR * 8) + 192 * 8, pb, SC1);
      if (__builtin_popcount((unsigned)__builtin_amdgcn_ballot_w64(ml[1] == T) & 0xffu) >= LIGHT) break;
      __builtin_amdgcn_s_sleep(8);
    }
    NOUNROLL
    for (unsigned spin = 0; spin < SPIN_CAP; ++spin) {
      asm volatile("" ::: "memory");
#pragma unroll
      for (int s = 0; s < 8; ++s) g[s] = __builtin_amdgcn_raw_buffer_load_b128(rwb, vo, pb + s * (PSTR * 8), SC1);
      ml = __builtin_amdgcn_raw_buffer_load_b128(rwb, (lane & 7) * (PSTR * 8) + 192 * 8, pb, SC1);
      unsigned bad = (ml[1] ^ T) | (ml[3] ^ T);
#pragma unroll
      for (int s = 0; s < 8; ++s) bad |= (g[s][1] ^ T) | (g[s][3] ^ T);
      if (__all(bad == 0u)) break;
      __builtin_amdgcn_s_sleep(2);
    }
    const float ms = __uint_as_float(ml[0]), ls = __uint_as_float(ml[2]);
    float M = fmaxf(ms, dpp<XOR1>(ms)); M = fmaxf(M, dpp<XOR2>(M)); M = fmaxf(M, dpp<HMIR>(M));
    const float wt = __builtin_amdgcn_exp2f((ms - M) * LOG2E);
    float Ls = wt * ls; Ls += dpp<XOR1>(Ls); Ls += dpp<XOR2>(Ls); Ls += dpp<HMIR>(Ls);
    const float wn = wt * __builtin_amdgcn_rcpf(Ls);
    float o0 = 0.f, o1 = 0.f;
#pragma unroll
    for (int s = 0; s < 8; ++s) { const float c = rdl(wn, s); o0 = fmaf(c, __uint_as_float(g[s][0]), o0); o1 = fmaf(c, __uint_as_float(g[s][2]), o1); }
    if (lane < 48) *(unsigned*)&L.ob[b][2 * d2] = f2bf(o0) | (f2bf(o1) << 16);
  }
  STAMP(8);
  BAR();
  if (w < 5) {
    v4f c = {0.f, 0.f, 0.f, 0.f};
#pragma unroll
    for (int ks = 0; ks < 6; ++ks) {
      const v4u a = *(const v4u*)&L.ob[lane & 1][32 * ks + 8 * (lane >> 4)];
      c = __builtin_amdgcn_mfma_f32_16x16x32_bf16(__builtin_bit_cast(bf16x8, a), __builtin_bit_cast(bf16x8, __builtin_shufflevector(wof[ks][0], wof[ks][1], 0, 1, 2, 3, 4, 5, 6, 7)), c, 0, 0, 0);
    }
    if (lane < 32) { const int b = lane >> 4; st_gran(ws, WS_Y + ((h2 * 2 + b) * DM + 80 * c2 + 16 * w + (lane & 15)) * 8, __float_as_uint(b ? c[1] : c[0]), T); }
  }
  STAMP(9);

  if (w == 7) {
    const unsigned yb = WS_Y + (unsigned)((l5 * 2 + b3) * DM + n0) * 8;
    u64 y0 = 0, y1 = 0, y2 = 0;
    NOUNROLL
    for (unsigned spin = 0; spin < SPIN_CAP; ++spin) {
      y0 = ld_gran(ws, yb + hf * 8); y1 = ld_gran(ws, yb + (2 + hf) * 8); y2 = ld_gran(ws, yb + 32);
      if (__all((unsigned)(y0 >> 32) == T && (unsigned)(y1 >> 32) == T && (unsigned)(y2 >> 32) == T)) break;
    }
    float a0 = row_sum(__uint_as_float((unsigned)y0)), a1 = row_sum(__uint_as_float((unsigned)y1)), a2 = row_sum(__uint_as_float((unsigned)y2));
    a0 = swap16_sum(a0, a0); a1 = swap16_sum(a1, a1); a2 = swap16_sum(a2, a2);
    const float h0 = rbf(bf2f(xres0) + rbf(a0)), h1 = rbf(bf2f(xres1) + rbf(a1)), h4 = rbf(bf2f(xres2) + rbf(a2));
    const float m5 = (swap32_sum(h0 + h1, h0 + h1) + h4) * 0.2f;
    const float d0 = h0 - m5, d1 = h1 - m5, d4 = h4 - m5, dd = fmaf(d0, d0, d1 * d1);
    const float q5 = fmaf(d4, d4, swap32_sum(dd, dd));
    if (lane == 0) __builtin_amdgcn_raw_buffer_store_b128(v4u{__float_as_uint(m5), T, __float_as_uint(q5), T}, rwb, (j * 32 + r) * 16, WS_H, SC1);
    STAMP(10);
    v4u ga, gb;
    NOUNROLL
    for (unsigned spin = 0; spin < SPIN_CAP; ++spin) {
      asm volatile("" ::: "memory");
      ga = __builtin_amdgcn_raw_buffer_load_b128(rwb, lane * 16, WS_H + b3 * 2048, SC1);
      gb = __builtin_amdgcn_raw_buffer_load_b128(rwb, lane * 16 + 1024, WS_H + b3 * 2048, SC1);
      if (__all(((ga[1] ^ T) | (ga[3] ^ T) | (gb[1] ^ T) | (gb[3] ^ T)) == 0u)) break;
    }
    STAMP(11);
    const float c0 = __int_as_float(__builtin_amdgcn_readfirstlane((int)ga[0]));
    const float ea = __uint_as_float(ga[0]) - c0, eb = __uint_as_float(gb[0]) - c0;
    float u = swap32_sum(ea + eb, fmaf(5.f, fmaf(ea, ea, eb * eb), __uint_as_float(ga[2]) + __uint_as_float(gb[2])));
    u = row_sum(u); u = swap16_sum(u, u);
    const float dm = rdl(u, 0) * (1.f / 128.f);
    const float mean = c0 + dm, var = fmaxf(fmaf(-dm, dm, rdl(u, 32) * (1.f / DM)), 0.f);
    const float mb = rbf(mean), inv = __builtin_amdgcn_rcpf(rbf(__builtin_amdgcn_sqrtf(rbf(rbf(var) + 1e-5f))));
    const float mb2 = rbf((mean - mb) * inv), inv2 = __builtin_amdgcn_rcpf(rbf(__builtin_amdgcn_sqrtf(rbf(rbf(var * inv * inv) + 1e-5f))));
    const float hm = l5 == 0 ? h0 : (l5 == 1 ? h1 : h4);
    const float cur = rbf(rbf(rbf(rbf(hm - mb) * inv) - mb2) * inv2);
    if (l5 < 3) { L.t[2 * l5 + hf] = cur; L.hv[2 * l5 + hf] = hm; }
    if (bid == 0 && lane == 0) __hip_atomic_store((GAS unsigned*)(ws + WS_EPOCH), T, RLX_AGENT);
  }
  const float fw1[3] = {bf2f(mw1[0]), bf2f(mw1[1]), bf2f(mw1[2])}, fb1[3] = {bf2f(mb1[0]), bf2f(mb1[1]), bf2f(mb1[2])}, fw2[3] = {bf2f(mw2[0]), bf2f(mw2[1]), bf2f(mw2[2])};
  BAR();
  STAMP(12);
  {
    constexpr float GA = -0.7978845608028654f * 2.f * LOG2E, GB = GA * 0.044715f;
    const v4f t03 = *(const v4f*)&L.t[0]; const v2f t45 = *(const v2f*)&L.t[4];
    v2f tp[3] = {v2f{t03[0], t03[1]}, v2f{t03[2], t03[3]}, t45}, a[3];
#pragma unroll
    for (int e = 0; e < 3; ++e) {
      a[e] = v2f{0.f, 0.f};
#pragma unroll
      for (int i = 0; i < 3; ++i) {
        const v2f u = pkfma(tp[e], v2f{fw1[i], fw1[i]}, v2f{fb1[i], fb1[i]});
        const v2f z = u * pkfma(u * u, v2f{GB, GB}, v2f{GA, GA});
        v2f rc;
        rc[0] = __builtin_amdgcn_rcpf(1.f + __builtin_amdgcn_exp2f(z[0]));
        rc[1] = e < 2 ? __builtin_amdgcn_rcpf(1.f + __builtin_amdgcn_exp2f(z[1])) : 0.f;
        a[e] = pkfma(u * v2f{fw2[i], fw2[i]}, rc, a[e]);
      }
    }
    float s = swap16_sum(swap32_sum(a[0][0], a[0][1]), swap32_sum(a[1][0], a[1][1]));
    float s4 = swap32_sum(a[2][0], a[2][0]); s4 = swap16_sum(s4, s4);
    s = row_sum(s); s4 = row_sum(s4);
    if ((lane & 15) == 0) L.red[w][(lane >> 5) + ((lane >> 3) & 2)] = s;
    if (lane == 1) L.red[w][4] = s4;
    BAR();
    if (tid < 5) {
      float y = 0.f;
#pragma unroll
      for (int i = 0; i < 8; ++i) y += L.red[i][tid];
      y = rbf(rbf(y) + bf2f(b2v));
      out[b3 * DM + n0 + tid] = (u16)f2bf(L.hv[tid] + y);
    }
  }
  STAMP(13);
}
}

extern "C" void kernel_launch(void* const* d_in, const int* in_sizes, int n_in, void* d_out, int out_size, void* d_ws, size_t ws_size,
                              hipStream_t stream) {
  using namespace fk;
  hipLaunchKernelGGL(fused, dim3(256), dim3(512), 0, stream, (const u16*)d_in[0], (const char*)d_in[1], (const char*)d_in[2], (const char*)d_in[3],
                     (const u16*)d_in[4], (const u16*)d_in[6], (const u16*)d_in[7], (const u16*)d_in[8], (const u16*)d_in[9], (u16*)d_out, (char*)d_ws);
}
```

```cpp
#include <hip/hip_runtime.h>
#include <stdint.h>

#define LOCK 2
#define CODE_KB 18
#define THR 3
#define LIGHT 7
#define KEARLY 2
#define XST "sc1 nt"
#define PACE 113
#define PACE_T0 620
#define PMODE 1
#define YSLEEP 1
#define HSLEEP 1
#define PSLEEP 2
#define STAMPS 0

namespace fk {
typedef unsigned short u16;
typedef unsigned long long u64;
typedef unsigned v2u __attribute__((ext_vector_type(2)));
typedef unsigned v3u __attribute__((ext_vector_type(3)));
typedef unsigned v4u __attribute__((ext_vector_type(4)));
typedef int v4i __attribute__((ext_vector_type(4)));
typedef float v4f __attribute__((ext_vector_type(4)));
typedef float v2f __attribute__((ext_vector_type(2)));
typedef short v4s __attribute__((ext_vector_type(4)));
typedef __bf16 bf16x2 __attribute__((ext_vector_type(2)));
typedef __bf16 bf16x8 __attribute__((ext_vector_type(8)));
#define GAS __attribute__((address_space(1)))
#define LAS __attribute__((address_space(3)))
#define RLX_AGENT __ATOMIC_RELAXED, __HIP_MEMORY_SCOPE_AGENT
#define NOUNROLL _Pragma("clang loop unroll(disable)")

constexpr int DM = 640, NH = 32, SK = 8192, DH = 192;
constexpr unsigned ROWB = 384, REGION = SK * ROWB, KVBYTES = 64u * REGION;
constexpr unsigned OOB = 0x40000000u;
constexpr int SC1 = 16;
constexpr unsigned WS_EPOCH = 0;
constexpr unsigned WS_PROBE = 4096;
constexpr unsigned WS_Q = 8192;
constexpr unsigned WS_H = WS_Q + 2 * 2 * 6144 * 8;
constexpr unsigned WS_Y = WS_H + 2 * 40 * 128;
constexpr unsigned PSTR = 208;
constexpr unsigned WS_P = WS_Y + 32 * 2 * 640 * 8;
constexpr unsigned WS_S = WS_P + 32 * 2 * 8 * PSTR * 8;
constexpr unsigned WS_END = WS_S + 64 * 8 * 8 * 384;
constexpr unsigned WS_STAMP = 8u << 20;
constexpr unsigned TAG_STEP = 0x9E3779B9u;
constexpr unsigned SPIN_CAP = 1u << 17;
constexpr float LOG2E = 1.4426950408889634f;

__device__ __forceinline__ float bf2f(unsigned v) { return __uint_as_float(v << 16); }
__device__ __forceinline__ unsigned f2bf(float f) { return (unsigned)__builtin_bit_cast(u16, (__bf16)f); }
__device__ __forceinline__ float rbf(float f) { return bf2f(f2bf(f)); }
__device__ __forceinline__ float lo16(unsigned v) { return __uint_as_float(v << 16); }
__device__ __forceinline__ float hi16(unsigned v) { return __uint_as_float(v & 0xffff0000u); }
__device__ __forceinline__ v2f unpk(unsigned v) { return v2f{lo16(v), hi16(v)}; }
__device__ __forceinline__ v2f pkfma(v2f a, v2f b, v2f c) { return __builtin_elementwise_fma(a, b, c); }
__device__ __forceinline__ float dot2(unsigned a, unsigned b, float c) {
  return __builtin_amdgcn_fdot2_f32_bf16(__builtin_bit_cast(bf16x2, a), __builtin_bit_cast(bf16x2, b), c, false);
}
template <int CTRL> __device__ __forceinline__ float dpp(float x) {
  return __builtin_bit_cast(float, __builtin_amdgcn_mov_dpp(__builtin_bit_cast(int, x), CTRL, 0xf, 0xf, true));
}
constexpr int XOR1 = 0xB1, XOR2 = 0x4E, HMIR = 0x141, MIR = 0x140, ROR4 = 0x124, ROR8 = 0x128;
__device__ __forceinline__ float row_sum(float x) {
  x += dpp<XOR1>(x); x += dpp<XOR2>(x); x += dpp<HMIR>(x); x += dpp<MIR>(x); return x;
}
__device__ __forceinline__ float row_max(float x) {
  x = fmaxf(x, dpp<XOR1>(x)); x = fmaxf(x, dpp<XOR2>(x)); x = fmaxf(x, dpp<HMIR>(x)); x = fmaxf(x, dpp<MIR>(x)); return x;
}
__device__ __forceinline__ float swap16_sum(float a, float b) {
  auto s = __builtin_amdgcn_permlane16_swap(__float_as_uint(a), __float_as_uint(b), false, false);
  return __uint_as_float(s[0]) + __uint_as_float(s[1]);
}
__device__ __forceinline__ float swap32_sum(float a, float b) {
  auto s = __builtin_amdgcn_permlane32_swap(__float_as_uint(a), __float_as_uint(b), false, false);
  return __uint_as_float(s[0]) + __uint_as_float(s[1]);
}
__device__ __forceinline__ float wave_sum(float x) { x = row_sum(x); x = swap16_sum(x, x); return swap32_sum(x, x); }
__device__ __forceinline__ float wave_max(float x) {
  x = row_max(x);
  auto s = __builtin_amdgcn_permlane16_swap(__float_as_uint(x), __float_as_uint(x), false, false);
  x = fmaxf(__uint_as_float(s[0]), __uint_as_float(s[1]));
  auto t = __builtin_amdgcn_permlane32_swap(__float_as_uint(x), __float_as_uint(x), false, false);
  return fmaxf(__uint_as_float(t[0]), __uint_as_float(t[1]));
}
__device__ __forceinline__ float rdl(float x, int l) { return __int_as_float(__builtin_amdgcn_readlane(__float_as_int(x), l)); }
__device__ __forceinline__ v4i mk_rsrc(const void* p, unsigned bytes) {
  const u64 a = (u64)p;
  v4i r; r[0] = (int)(unsigned)a; r[1] = (int)((a >> 32) & 0xffff); r[2] = (int)bytes; r[3] = 0x00020000; return r;
}
__device__ __forceinline__ void st_gran(char* ws, unsigned off, unsigned val, unsigned tag) {
  __hip_atomic_store((GAS u64*)(ws + off), ((u64)tag << 32) | val, RLX_AGENT);
}
__device__ __forceinline__ u64 ld_gran(char* ws, unsigned off) { return __hip_atomic_load((GAS u64*)(ws + off), RLX_AGENT); }
#define BAR() do { asm volatile("s_waitcnt lgkmcnt(0)" ::: "memory"); __builtin_amdgcn_s_barrier(); asm volatile("" ::: "memory"); } while (0)
__device__ __forceinline__ unsigned fcode(unsigned off) { return (((off >> 19) & 1u) << 2) ^ ((off >> 20) & 3u); }
#define STAMP(i) do { } while (0)

struct __attribute__((aligned(16))) Lds {
  float xn[DM][2];
  float qp[8][128];
  unsigned q[64][4];
  float p[8][2][96];
  unsigned sc[2][8][80];
  float ow[2][8][200];
  float pl[2][8][200];
  float wm[8];
  u16 ob[2][192];
  float t[8];
  float hv[8];
  float red[8][8];
  unsigned flag, flag_pad[3];
  u16 wt[192 * 80];
  char pad[15 * 1024];
};

#define LOADBLK(R, rs, so, so5) asm volatile("s_nop 4\n\t" \
    "buffer_load_dwordx3 %0, %6, %8, %9 offen nt\n\t" \
    "buffer_load_dwordx3 %1, %6, %8, %9 offen offset:768 nt\n\t" \
    "buffer_load_dwordx3 %2, %6, %8, %9 offen offset:1536 nt\n\t" \
    "buffer_load_dwordx3 %3, %6, %8, %9 offen offset:2304 nt\n\t" \
    "buffer_load_dwordx3 %4, %6, %8, %9 offen offset:3072 nt\n\t" \
    "buffer_load_dwordx3 %5, %7, %8, %10 offen offset:3840 nt" \
    : "=&v"(R[0]), "=&v"(R[1]), "=&v"(R[2]), "=&v"(R[3]), "=&v"(R[4]), "=&v"(R[5]) \
    : "v"(voff), "v"(voff5), "s"(rs), "s"(so), "s"(so5) : "memory")
#define WAITV(n, r) asm volatile("s_waitcnt vmcnt(%1)" : "+v"(r) : "i"(n) : "memory")
#define LOCKSTEP(d, n) do { if (LOCK && ((d) % (LOCK ? LOCK : 1)) == LOCK - 1) { \
    if (PACE && w == 7 && (n) < 43u) { const unsigned due = tbase + ((n) + 1u) * pace; \
      NOUNROLL for (int sp_ = 0; sp_ < 4096 && (int)((unsigned)__builtin_amdgcn_s_memrealtime() - due) < 0; ++sp_) __builtin_amdgcn_s_sleep(1); } \
    __builtin_amdgcn_s_barrier(); } } while (0)
__global__ void __launch_bounds__(512, 2)
fused(const u16* __restrict__ x, const char* __restrict__ K, const char* __restrict__ V, const char* __restrict__ wq, const u16* __restrict__ wo,
      const u16* __restrict__ w1, const u16* __restrict__ b1, const u16* __restrict__ w2, const u16* __restrict__ b2, u16* __restrict__ out, char* ws) {
  __shared__ Lds L;
  const int tid = threadIdx.x, lane = tid & 63, w = __builtin_amdgcn_readfirstlane(tid >> 6);
  const int bid = blockIdx.x, j = bid & 7, r = bid >> 3;
  const int hf = lane >> 5, l5 = lane & 31;
  STAMP(0);
  unsigned tbase = PACE ? (unsigned)__builtin_amdgcn_s_memrealtime() + PACE_T0 : 0u, pace = PACE;
  unsigned rnd = (unsigned)(__builtin_amdgcn_s_memtime() >> 2);
  unsigned T;
  asm volatile("s_load_dword %0, %2, 0x0" : "=s"(T), "+s"(rnd) : "s"(ws) : "memory");
  const unsigned xcc = (unsigned)__builtin_amdgcn_s_getreg(20 | (3 << 11)) & 7u;
  const __amdgpu_buffer_rsrc_t rwb = __builtin_amdgcn_make_buffer_rsrc((void*)ws, 0, WS_END, 0x00020000);
  const v4i rw = mk_rsrc(ws, WS_END);
  const int h2 = 4 * j + (r >> 3), c2 = r & 7;
  const int b3 = j >> 2, n0 = ((j & 3) * 32 + r) * 5;

  v4u xs4 = {0u, 0u, 0u, 0u}; unsigned xs1 = 0;
  if (w == 0) {
    const int rg = xcc & 3;
    const unsigned off = (unsigned)((rg & 1) * 32 + r) * REGION + (32u + (rnd & 31u)) * 32768u;
    const unsigned pa = fcode(off);
    const char* base = ((rg & 2) ? V : K) + off + ((rnd >> 5) & 31u) * 128u;
    const char* A = base + pa * 4096;
    const char* B = base + (pa ^ 2u) * 4096;
    u64 t1, t2; unsigned va, vb; const unsigned z = 0;
    asm volatile("global_load_dword %2, %4, %5 nt\n\t"
                 "global_load_dword %3, %4, %6 nt\n\t"
                 "s_waitcnt vmcnt(1)\n\ts_memtime %0\n\t"
                 "s_waitcnt vmcnt(0)\n\ts_memtime %1\n\t"
                 "s_waitcnt lgkmcnt(0)" : "=&s"(t1), "=&s"(t2), "=&v"(va), "=&v"(vb) : "v"(z), "s"(A), "s"(B) : "memory");
    asm volatile("" : "+s"(T));
    const unsigned gap = min((unsigned)(t2 - t1), 0xffffffu);
    if (lane == 0) { st_gran(ws, WS_PROBE + r * 128 + j * 8, (gap << 8) | xcc, T + TAG_STEP); L.flag = 0u; }
  } else {
    __builtin_amdgcn_s_sleep(4);
    if (w <= 3) {
      u64 pc; asm volatile("s_getpc_b64 %0" : "=s"(pc));
      const char* xp = w < 3 ? (const char*)(x + (w - 1) * DM + lane * 10) : (const char*)(pc & ~255ull) + (r < CODE_KB ? r : 0) * 1024 + lane * 16;
      asm volatile("global_load_dwordx4 %0, %2, off\n\tglobal_load_dword %1, %2, off offset:16" : "=&v"(xs4), "=&v"(xs1) : "v"(xp) : "memory");
    }
  }
  auto mk_tab = [&](unsigned lab4, unsigned first) {
    const unsigned ph = ((unsigned)lane * 43u) >> 9;
    const unsigned per = ((unsigned)lane - 12u * ph) * 8u + (unsigned)w;
    const unsigned rbase = ((ph & 1u) * 32u + (unsigned)r) * REGION;
    const unsigned k = per * 8u + ((lane < 4 ? first : (lab4 >> ((8u * ph) & 31u)) & 7u) ^ fcode(rbase + per * 32768u));
    const unsigned r0 = ((32u * k + 1u) * 43691u) >> 17, r1 = ((32u * k + 33u) * 43691u) >> 17;
    return lane < 48 ? rbase + r0 * ROWB + (r1 - r0 - 10u) : OOB;
  };
  auto mk_lab = [&](unsigned id) { return (((id >> 1) ^ (unsigned)(r >> 1)) & 1u) | ((id >> 2) << 1) | (((id ^ (unsigned)r) & 1u) << 2); };
  const unsigned labF = mk_lab((unsigned)j) ^ ((unsigned)(r >> 1) & 2u);
  unsigned tabv = mk_tab(0u, labF);
  const v4i rk = mk_rsrc(K, KVBYTES), rv = mk_rsrc(V, KVBYTES);
  const unsigned voff = lane * 12, voff5 = hf ? OOB : voff;
  v3u ring[4][6];
  auto issue = [&](int d, unsigned rev, const v4i& rs) {
    const unsigned v = (unsigned)__builtin_amdgcn_readlane((int)tabv, (int)(rev * 4u) + d);
    const unsigned so = v & ~1u, so5 = (v & 1u) ? so : OOB;
    LOADBLK(ring[d], rs, so, so5);
  };
  v4u wqv[5];
  {
    const v4i rq = mk_rsrc(wq, DM * NH * DH * 2);
    const unsigned so = (r < 24 ? 0u : OOB) + (unsigned)(320 * (r & 1) + 40 * w) * (NH * DH * 2) + (12 * j + (r >> 1)) * 128;
    const unsigned vq = (lane >> 3) * (NH * DH * 2) + (lane & 7) * 16;
#pragma unroll
    for (int i = 0; i < 5; ++i) { const unsigned si = so + i * 8 * (NH * DH * 2); asm volatile("buffer_load_dwordx4 %0, %1, %2, %3 offen nt" : "=&v"(wqv[i]) : "v"(vq), "s"(rq), "s"(si) : "memory"); }
  }
  asm volatile("s_waitcnt lgkmcnt(0)" : "+s"(T) :: "memory");
  T += TAG_STEP;
  STAMP(1);
  unsigned vo[4];
#pragma unroll
  for (int i = 0; i < 4; ++i) { const unsigned c = tid + 512 * i, row = (c * 6554u) >> 16; vo[i] = c * 16u + row * (DM * 2 - 160); }
  if (tid >= 384) vo[3] = OOB;
  asm volatile("" : "+v"(vo[0]), "+v"(vo[1]), "+v"(vo[2]), "+v"(vo[3]));
  if (w != 0) asm volatile("s_waitcnt vmcnt(%2)" : "+v"(xs4), "+v"(xs1) : "i"(KEARLY == 1 ? 11 : 5) : "memory");
  if (w == 1 || w == 2) {
    float e[10];
#pragma unroll
    for (int i = 0; i < 4; ++i) { e[2 * i] = lo16(xs4[i]); e[2 * i + 1] = hi16(xs4[i]); }
    e[8] = lo16(xs1); e[9] = hi16(xs1);
    const float c0 = __int_as_float(__builtin_amdgcn_readfirstlane(__float_as_int(e[0])));
    float s = 0.f, v = 0.f;
#pragma unroll
    for (int i = 0; i < 10; ++i) { const float d = e[i] - c0; s += d; v = fmaf(d, d, v); }
    float u = swap32_sum(s, v); u = row_sum(u); u = swap16_sum(u, u);
    const float dm = rdl(u, 0) * (1.f / DM), var = fmaxf(fmaf(-dm, dm, rdl(u, 32) * (1.f / DM)), 0.f);
    const float mb = rbf(c0 + dm), inv = 1.f / rbf(sqrtf(rbf(rbf(var) + 1e-5f)));
#pragma unroll
    for (int i = 0; i < 10; ++i) L.xn[lane * 10 + i][w - 1] = rbf(rbf(e[i] - mb) * inv);
  }
  BAR();
  STAMP(14);
  v2u pg; v4u wov[4];
  const unsigned sp = WS_PROBE + r * 128, vp = (lane & 7) * 8;
  {
    v2f acc[2][4];
#pragma unroll
    for (int c = 0; c < 4; ++c) { acc[0][c] = v2f{0.f, 0.f}; acc[1][c] = v2f{0.f, 0.f}; }
#pragma unroll
    for (int i = 0; i < 5; ++i) {
      const v2f xv = *(const v2f*)L.xn[320 * (r & 1) + 40 * w + 8 * i + (lane >> 3)];
      WAITV((KEARLY == 2 ? 4 : 10) - i, wqv[i]);
      if (KEARLY == 2 && i == 4) issue(0, 0u, rk);
#pragma unroll
      for (int c = 0; c < 4; ++c) {
        const v2f a = unpk(wqv[i][c]);
        acc[0][c] = pkfma(a, v2f{xv[0], xv[0]}, acc[0][c]);
        acc[1][c] = pkfma(a, v2f{xv[1], xv[1]}, acc[1][c]);
      }
    }
    asm volatile("" : "+v"(acc[0][0]), "+v"(acc[0][1]), "+v"(acc[0][2]), "+v"(acc[0][3]), "+v"(acc[1][0]), "+v"(acc[1][1]), "+v"(acc[1][2]), "+v"(acc[1][3]));
  {
    const v4i ro = mk_rsrc(wo, DM * NH * DH * 2);
    const unsigned so = (unsigned)(h2 * DH * DM + 80 * c2) * 2;
    asm volatile("buffer_load_dwordx2 %0, %5, %10, %11 offen sc1\n\t"
                 "buffer_load_dwordx4 %1, %6, %12, %13 offen nt\n\tbuffer_load_dwordx4 %2, %7, %12, %13 offen nt\n\t"
                 "buffer_load_dwordx4 %3, %8, %12, %13 offen nt\n\tbuffer_load_dwordx4 %4, %9, %12, %13 offen nt"
                 : "=&v"(pg), "=&v"(wov[0]), "=&v"(wov[1]), "=&v"(wov[2]), "=&v"(wov[3])
                 : "v"(vp), "v"(vo[0]), "v"(vo[1]), "v"(vo[2]), "v"(vo[3]), "s"(rw), "s"(sp), "s"(ro), "s"(so) : "memory");
  }
    float s1[8], s2[4];
#pragma unroll
    for (int c = 0; c < 8; ++c) s1[c] = swap32_sum(acc[0][c >> 1][c & 1], acc[1][c >> 1][c & 1]);
#pragma unroll
    for (int c = 0; c < 4; ++c) { float t = swap16_sum(s1[c], s1[c + 4]); t += dpp<ROR8>(t); s2[c] = t; }
    if ((lane & 8) == 0) *(v4f*)&L.qp[w][hf * 64 + 8 * (lane & 7) + 4 * ((lane >> 4) & 1)] = v4f{s2[0], s2[1], s2[2], s2[3]};
  }
  STAMP(15);
  BAR();
  if (r < 24 && w == 0) {
    v2f a = *(const v2f*)&L.qp[0][2 * lane];
#pragma unroll
    for (int i = 1; i < 8; ++i) a += *(const v2f*)&L.qp[i][2 * lane];
    __builtin_amdgcn_raw_buffer_store_b128(v4u{__float_as_uint(a[0]), T, __float_as_uint(a[1]), T}, rwb, hf * (6144 * 8) + l5 * 16,
                                           WS_Q + (r & 1) * (2 * 6144 * 8) + (12 * j + (r >> 1)) * 512, SC1);
  }
  STAMP(2);

  issue(1, 0u, rk);
  unsigned labs;
  {
    WAITV(10, pg);
    NOUNROLL
    for (unsigned spin = 0; !__all(pg[1] == T) && spin < SPIN_CAP; ++spin) {
      __builtin_amdgcn_s_sleep(1);
      asm volatile("buffer_load_dwordx2 %0, %1, %2, %3 offen sc1\n\ts_waitcnt vmcnt(0)" : "=&v"(pg) : "v"(vp), "s"(rw), "s"(sp) : "memory");
    }
    const int gx = (int)pg[0] & 7;
    int msk = 1 << gx;
    msk |= __builtin_amdgcn_mov_dpp(msk, XOR1, 0xf, 0xf, true); msk |= __builtin_amdgcn_mov_dpp(msk, XOR2, 0xf, 0xf, true); msk |= __builtin_amdgcn_mov_dpp(msk, HMIR, 0xf, 0xf, true);
    const unsigned gv = (unsigned)__builtin_amdgcn_ds_permute(gx * 4, (int)pg[0]) >> 8;
    const unsigned go = (unsigned)__builtin_amdgcn_mov_dpp((int)gv, 0x104, 0xf, 0xf, true);
    const unsigned far = (unsigned)__builtin_amdgcn_ballot_w64(go > gv) & 15u;
    labs = labF * 0x01010101u;
    if (__builtin_amdgcn_readfirstlane(msk) == 0xff)
      labs = (mk_lab(xcc) * 0x01010101u) ^ 0x04000400u ^ ((far & 1u) << 1) ^ ((far & 2u) << 8) ^ ((far & 4u) << 15) ^ ((far & 8u) << 22);
  }
  tabv = mk_tab(labs, labF);
  const u64 n11 = __builtin_amdgcn_ballot_w64((tabv & 1u) != 0u);
  STAMP(3);
  const int g3 = (j & 3) * 10 + r;
  unsigned voc = OOB;
  if (tid < 256) { if ((tid & 127) < 97) voc = WS_P + ((r * 2 + (tid < 128)) * 8 + ((labs >> (tid < 128 ? 24 : 16)) & 7u)) * (PSTR * 8) + (tid & 127) * 16; }
  else if (tid < 336) voc = WS_Y + ((h2 * 2 + (tid >= 296)) * DM + 80 * c2) * 8 + (tid >= 296 ? tid - 296 : tid - 256) * 16;
  else if (tid >= 448 && tid < 453 && r < 10) voc = WS_H + (b3 * 40 + g3) * 128 + (tid - 448) * 16;
  v4u old;
  asm volatile("buffer_load_dwordx4 %0, %1, %2, 0 offen sc1 nt" : "=&v"(old) : "v"(voc), "s"(rw) : "memory");
  v2u mw1, mb1, mw2; unsigned xres;
  {
    const unsigned vm = (unsigned)tid * 6u & ~3u;
    const u16* xr = w == 7 ? x + b3 * DM + min(g3, 39) * 16 + 2 * (lane & 7) + hf : b2;
    asm volatile("global_load_dwordx2 %0, %4, %5\n\tglobal_load_dwordx2 %1, %4, %6\n\tglobal_load_dwordx2 %2, %4, %7\n\tglobal_load_ushort %3, %8, off"
                 : "=&v"(mw1), "=&v"(mb1), "=&v"(mw2), "=&v"(xres) : "v"(vm), "s"(w1), "s"(b1), "s"(w2), "v"(xr) : "memory");
  }
  {
    volatile LAS unsigned* flag = (volatile LAS unsigned*)&L.flag;
    bool have = w != 0;
    auto poll = [&]() {
      asm volatile("" ::: "memory");
      v4u g[6]; unsigned bad = 0;
#pragma unroll
      for (int i = 0; i < 6; ++i) g[i] = __builtin_amdgcn_raw_buffer_load_b128(rwb, hf * (6144 * 8) + l5 * 48 + (i % 3) * 16, WS_Q + (i / 3) * (2 * 6144 * 8) + r * (DH * 8), SC1);
#pragma unroll
      for (int i = 0; i < 6; ++i) bad |= (g[i][1] ^ T) | (g[i][3] ^ T);
      have = __all(bad == 0u);
      if (have) {
#pragma unroll
        for (int i = 0; i < 3; ++i) L.q[lane][i] = f2bf(__uint_as_float(g[i][0]) + __uint_as_float(g[i + 3][0])) | (f2bf(__uint_as_float(g[i][2]) + __uint_as_float(g[i + 3][2])) << 16);
        *flag = 1u;
      }
    };
#pragma unroll
    for (int d = 2; d < 4; ++d) {
      if (!have) poll();
      else if (__builtin_amdgcn_readfirstlane(*flag) == 0u) asm volatile("s_waitcnt vmcnt(%0)" :: "i"(THR) : "memory");
      issue(d, 0u, rk);
    }
    NOUNROLL
    for (unsigned spin = 0; !have && spin < SPIN_CAP; ++spin) { poll(); if (!have) __builtin_amdgcn_s_sleep(1); }
  }
  asm volatile("s_waitcnt vmcnt(12)" : "+v"(wov[0]), "+v"(wov[1]), "+v"(wov[2]), "+v"(wov[3]), "+v"(old), "+v"(mw1), "+v"(mb1), "+v"(mw2), "+v"(xres) :: "memory");
#pragma unroll
  for (int i = 0; i < 4; ++i) if (i < 3 || tid < 384) *(v4u*)&L.wt[(tid + 512 * i) * 8] = wov[i];
  if (voc != OOB && (old[1] == T || old[3] == T)) __builtin_amdgcn_raw_buffer_store_b128(v4u{0u, ~T, 0u, ~T}, rwb, voc, 0, SC1);
  BAR();
  STAMP(4);

  const bool slot_ok = (l5 & 15) < 12;
  const int slot = (l5 >> 4) * 12 + (l5 & 15);
  auto srec = [&](unsigned b, unsigned lab) { return WS_S + (((b * 32u + (unsigned)r) * 8u + lab) * 8u + (unsigned)w) * 384u + (unsigned)lane * 16u; };
  const unsigned lk0 = lane < 8 ? labF : labs & 7u, lk1 = (labs >> 8) & 7u, lv0 = (labs >> 16) & 7u, lv1 = labs >> 24;
  const bool x0 = lane < 24 && lk0 != lv0, x1 = lane < 24 && lk1 != lv1;
  const unsigned cross = (__builtin_amdgcn_ballot_w64(x0) != 0ull ? 1u : 0u) | (lk1 != lv1 ? 2u : 0u);
  v4u sg = {0u, 0u, 0u, 0u};
#define SLOAD(vo) asm volatile("buffer_load_dwordx4 %0, %1, %2, 0 offen sc1 nt" : "+v"(sg) : "v"(vo), "s"(rw) : "memory")

  NOUNROLL
  for (unsigned rev = 0; rev < 6; ++rev) {
    const unsigned ph = rev >= 3u;
    if (rev == 5u && (cross & 1u)) { const unsigned vo = x0 ? srec(0u, lv0) : OOB; SLOAD(vo); }
    const v3u qv = *(const v3u*)L.q[ph * 32 + l5];
    const v4i rs = rev == 5u ? rv : rk;
    const unsigned e4 = (unsigned)(n11 >> (rev * 4u));
    float col[2] = {0.f, 0.f};
#pragma unroll
    for (int d = 0; d < 4; ++d) {
#pragma unroll
      for (int u = 0; u < 6; ++u) {
        WAITV(23 - u, ring[d][u]);
        float s = dot2(ring[d][u][0], qv[0], 0.f);
        s = dot2(ring[d][u][1], qv[1], s);
        s = dot2(ring[d][u][2], qv[2], s);
        s = row_sum(s);
        if (u == 5) s = (hf == 0 && ((e4 >> d) & 1u)) ? s : -INFINITY;
        col[d >> 1] = ((lane & 15) == (d & 1) * 6 + u) ? s : col[d >> 1];
      }
      asm volatile("" : "+v"(col[d >> 1]));
      issue(d, rev + 1u, rs);
      LOCKSTEP(d, rev * 4u + d);
    }
    const float sc = rbf(swap16_sum(col[0], col[1])) * 0.07216878364870322f;
    if (slot_ok) ((u16*)&L.sc[ph][w][(rev - 3u * ph) * 24u + slot])[hf] = (u16)f2bf(sc);
    if (rev - 3u * ph == 2u && ((cross >> ph) & 1u)) {
      asm volatile("s_waitcnt lgkmcnt(0)" ::: "memory");
      const unsigned* e = &L.sc[ph][w][3 * (lane < 24 ? lane : 0)];
      const unsigned e0 = e[0], e1 = e[1], e2 = e[2];
      const v4u g = {e0, (e1 & 0xffffu) | (T << 16), e2, (e1 >> 16) | (T & 0xffff0000u)};
      const unsigned vo = (ph ? x1 : x0) ? srec(ph, ph ? lk1 : lk0) : OOB;
      asm volatile("buffer_store_dwordx4 %0, %1, %2, 0 offen " XST "\n\ts_nop 1" :: "v"(g), "v"(vo), "s"(rw) : "memory");
    }
  }
  STAMP(5);

  NOUNROLL
  for (unsigned b = 0; b < 2; ++b) {
    if ((cross >> b) & 1u) {
      WAITV(24, sg);
      const bool xb = b ? x1 : x0;
      const unsigned vo = xb ? srec(b, b ? lv1 : lv0) : OOB;
      NOUNROLL
      for (unsigned spin = 0; !__all(!xb || ((sg[1] >> 16) | (sg[3] & 0xffff0000u)) == T) && spin < SPIN_CAP; ++spin) {
        __builtin_amdgcn_s_sleep(2);
        SLOAD(vo);
        WAITV(0, sg);
      }
      if (xb) { unsigned* e = &L.sc[b][w][3 * lane]; e[0] = sg[0]; e[1] = (sg[1] & 0xffffu) | (sg[3] << 16); e[2] = sg[2]; }
    }
    asm volatile("s_waitcnt lgkmcnt(0)" ::: "memory");
    const u16* scp = (const u16*)&L.sc[b][w][slot] + hf;
    const unsigned ninf = 0xff80u, u0 = scp[0], u1 = scp[48], u2 = scp[96];
    const float s0 = bf2f(slot_ok ? u0 : ninf), s1 = bf2f(slot_ok ? u1 : ninf), s2 = bf2f(slot_ok ? u2 : ninf);
    const float mw = wave_max(fmaxf(fmaxf(s0, s1), s2));
    if (lane == 0) L.wm[w] = mw;
    BAR();
    const v4f ma = *(const v4f*)&L.wm[0], mb = *(const v4f*)&L.wm[4];
    const float M = fmaxf(fmaxf(fmaxf(ma[0], ma[1]), fmaxf(ma[2], ma[3])), fmaxf(fmaxf(mb[0], mb[1]), fmaxf(mb[2], mb[3])));
    const float p0 = __builtin_amdgcn_exp2f((s0 - M) * LOG2E), p1 = __builtin_amdgcn_exp2f((s1 - M) * LOG2E), p2 = __builtin_amdgcn_exp2f((s2 - M) * LOG2E);
    if (slot_ok) { const int q = l5 & 15; float* pp = &L.p[w][hf][(l5 >> 4) * 16 + (q >= 6 ? q + 2 : q)]; pp[0] = p0; pp[32] = p1; pp[64] = p2; }
    const float ls = wave_sum(p0 + p1 + p2);
    if (lane == 0) *(v2f*)&L.ow[b][w][192] = v2f{w == 0 ? M : 0.f, ls};
    v2f acc[3] = {v2f{0.f, 0.f}, v2f{0.f, 0.f}, v2f{0.f, 0.f}};
    asm volatile("s_waitcnt lgkmcnt(0)" ::: "memory");
    NOUNROLL
    for (unsigned t = 0; t < 3; ++t) {
      if (t == 2u && b == 0u && (cross & 2u)) { const unsigned vo = x1 ? srec(1u, lv1) : OOB; SLOAD(vo); }
      float pr[4][6];
#pragma unroll
      for (int d = 0; d < 4; ++d) {
        const v4f a = *(const v4f*)&L.p[w][hf][t * 32 + d * 8]; const v2f c = *(const v2f*)&L.p[w][hf][t * 32 + d * 8 + 4];
        pr[d][0] = a[0]; pr[d][1] = a[1]; pr[d][2] = a[2]; pr[d][3] = a[3]; pr[d][4] = c[0]; pr[d][5] = c[1];
      }
#pragma unroll
      for (int d = 0; d < 4; ++d) {
#pragma unroll
        for (int u = 0; u < 6; ++u) {
          WAITV(23 - u, ring[d][u]);
#pragma unroll
          for (int i = 0; i < 3; ++i) acc[i] = pkfma(unpk(ring[d][u][i]), v2f{pr[d][u], pr[d][u]}, acc[i]);
        }
        asm volatile("" : "+v"(acc[0]), "+v"(acc[1]), "+v"(acc[2]));
        issue(d, 7u + 3u * b + t, rv);
        if (d < 3 || t < 2u) LOCKSTEP(d, (6u + 3u * b + t) * 4u + d);
      }
    }
#pragma unroll
    for (int i = 0; i < 3; ++i) L.ow[b][w][6 * l5 + 2 * i + hf] = swap32_sum(acc[i][0], acc[i][1]);
  }
  WAITV(0, sg);
#pragma unroll
  for (int d = 0; d < 4; ++d)
    asm volatile("" :: "v"(ring[d][0]), "v"(ring[d][1]), "v"(ring[d][2]), "v"(ring[d][3]), "v"(ring[d][4]), "v"(ring[d][5]));
  STAMP(6);
  BAR();
  if (w < 4) {
    const float* op = &L.ow[tid < 128][0][2 * min(tid & 127, 96)];
    v2f v = *(const v2f*)op;
#pragma unroll
    for (int i = 1; i < 8; ++i) v += *(const v2f*)(op + 200 * i);
    __builtin_amdgcn_raw_buffer_store_b128(v4u{__float_as_uint(v[0]), T, __float_as_uint(v[1]), T}, rwb, voc, 0, SC1);
  }
  STAMP(7);

  {
    const unsigned pb = WS_P + (unsigned)(h2 * 16 + w) * (PSTR * 8);
    v4u g0, g1, g2, g3;
    NOUNROLL
    for (unsigned spin = 0; spin < SPIN_CAP; ++spin) {
      asm volatile("" ::: "memory");
      g3 = __builtin_amdgcn_raw_buffer_load_b128(rwb, hf * (8 * PSTR * 8) + 1536, pb, SC1);
      if (!__all(g3[1] == T)) { __builtin_amdgcn_s_sleep(PSLEEP); continue; }
      g0 = __builtin_amdgcn_raw_buffer_load_b128(rwb, lane * 16, pb + 8 * PSTR * 8, SC1);
      g1 = __builtin_amdgcn_raw_buffer_load_b128(rwb, lane * 16, pb, SC1);
      g2 = __builtin_amdgcn_raw_buffer_load_b128(rwb, hf * (8 * PSTR * 8) + l5 * 16 + 1024, pb, SC1);
      if (__all(((g0[1] ^ T) | (g0[3] ^ T) | (g1[1] ^ T) | (g1[3] ^ T) | (g2[1] ^ T) | (g2[3] ^ T) | (g3[1] ^ T) | (g3[3] ^ T)) == 0u)) break;
      __builtin_amdgcn_s_sleep(PSLEEP);
    }
    *(v2u*)&L.pl[1][w][2 * lane] = v2u{g0[0], g0[2]};
    *(v2u*)&L.pl[0][w][2 * lane] = v2u{g1[0], g1[2]};
    *(v2u*)&L.pl[hf][w][128 + 2 * l5] = v2u{g2[0], g2[2]};
    if (l5 == 0) *(v2u*)&L.pl[hf][w][192] = v2u{g3[0], g3[2]};
  }
  v4s wof[6][2];
  {
    const int wc = w < 5 ? w : 0;
    const LAS u16* bp = (const LAS u16*)L.wt + (8 * (lane >> 4) + ((lane & 15) >> 2)) * 80 + 16 * wc + 4 * (lane & 3);
#pragma unroll
    for (int ks = 0; ks < 6; ++ks)
#pragma unroll
      for (int t = 0; t < 2; ++t) wof[ks][t] = __builtin_amdgcn_ds_read_tr16_b64_v4i16((LAS v4s*)(bp + (32 * ks + 4 * t) * 80));
  }
  STAMP(8);
  BAR();
  if (w < 4) {
    const int b = w >> 1, d2 = (w & 1) * 48 + (lane < 48 ? lane : 0);
    const v2f ml = *(const v2f*)&L.pl[b][lane & 7][192];
    v2f g[8];
#pragma unroll
    for (int s = 0; s < 8; ++s) g[s] = *(const v2f*)&L.pl[b][s][2 * d2];
    float M = fmaxf(ml[0], dpp<XOR1>(ml[0])); M = fmaxf(M, dpp<XOR2>(M)); M = fmaxf(M, dpp<HMIR>(M));
    const float wt = __builtin_amdgcn_exp2f((ml[0] - M) * LOG2E);
    float Ls = wt * ml[1]; Ls += dpp<XOR1>(Ls); Ls += dpp<XOR2>(Ls); Ls += dpp<HMIR>(Ls);
    const float wn = wt * __builtin_amdgcn_rcpf(Ls);
    v2f o = {0.f, 0.f};
#pragma unroll
    for (int s = 0; s < 8; ++s) { const float c = rdl(wn, s); o = pkfma(v2f{c, c}, g[s], o); }
    *(unsigned*)&L.ob[b][2 * d2] = f2bf(o[0]) | (f2bf(o[1]) << 16);
  }
  BAR();
  if (w < 5) {
    v4f c = {0.f, 0.f, 0.f, 0.f};
#pragma unroll
    for (int ks = 0; ks < 6; ++ks) {
      const v4u a = *(const v4u*)&L.ob[lane & 1][32 * ks + 8 * (lane >> 4)];
      c = __builtin_amdgcn_mfma_f32_16x16x32_bf16(__builtin_bit_cast(bf16x8, a), __builtin_bit_cast(bf16x8, __builtin_shufflevector(wof[ks][0], wof[ks][1], 0, 1, 2, 3, 4, 5, 6, 7)), c, 0, 0, 0);
    }
    const int b = (lane >> 4) & 1, n = 80 * c2 + 16 * w + (lane & 15);
    const unsigned yo = WS_Y + (unsigned)((h2 * 2 + b) * DM + n) * 8u;
    if (lane < 32) st_gran(ws, yo, __float_as_uint(b ? c[1] : c[0]), T);
  }
  STAMP(9);

  if (w == 7) {
    if (r < 10) {
      v4u y[4];
      NOUNROLL
      for (unsigned spin = 0; spin < SPIN_CAP; ++spin) {
        asm volatile("" ::: "memory");
        unsigned bad = 0;
#pragma unroll
        for (int i = 0; i < 4; ++i) y[i] = __builtin_amdgcn_raw_buffer_load_b128(rwb, (lane >> 3) * (2 * DM * 8) + (lane & 7) * 16, WS_Y + (16 * i + b3) * (DM * 8) + g3 * 128, SC1);
#pragma unroll
        for (int i = 0; i < 4; ++i) bad |= (y[i][1] ^ T) | (y[i][3] ^ T);
        if (__all(bad == 0u)) break;
        __builtin_amdgcn_s_sleep(YSLEEP);
      }
      const float e0 = (__uint_as_float(y[0][0]) + __uint_as_float(y[1][0])) + (__uint_as_float(y[2][0]) + __uint_as_float(y[3][0]));
      const float e1 = (__uint_as_float(y[0][2]) + __uint_as_float(y[1][2])) + (__uint_as_float(y[2][2]) + __uint_as_float(y[3][2]));
      float a = swap32_sum(e0, e1); a = swap16_sum(a, a); a += dpp<ROR8>(a);
      const unsigned hb = f2bf(bf2f(xres) + rbf(a));
      const float hv = bf2f(hb);
      float m = hv + dpp<XOR1>(hv); m += dpp<XOR2>(m); m += dpp<HMIR>(m); m = swap32_sum(m, m) * (1.f / 16.f);
      float q = (hv - m) * (hv - m); q += dpp<XOR1>(q); q += dpp<XOR2>(q); q += dpp<HMIR>(q); q = swap32_sum(q, q);
      auto sw = __builtin_amdgcn_permlane32_swap(hb, hb, false, false);
      const unsigned val = lane == 6 ? __float_as_uint(m) : (lane == 7 ? __float_as_uint(q) : sw[0] | (sw[1] << 16));
      __builtin_amdgcn_raw_buffer_store_b64(v2u{val, T}, rwb, lane >= 6 && lane < 16 ? (lane - 6) * 8 : OOB, WS_H + (b3 * 40 + g3) * 128, SC1);
    }
    STAMP(10);
    v4u ga; v2u gh;
    const int ne = n0 + min(2 * l5 + hf, 4);
    NOUNROLL
    for (unsigned spin = 0; spin < SPIN_CAP; ++spin) {
      asm volatile("" ::: "memory");
      ga = __builtin_amdgcn_raw_buffer_load_b128(rwb, min(lane, 39) * 128, WS_H + b3 * (40 * 128), SC1);
      gh = __builtin_amdgcn_raw_buffer_load_b64(rwb, (ne >> 4) * 128 + 16 + ((ne & 15) >> 1) * 8, WS_H + b3 * (40 * 128), SC1);
      if (__all(((ga[1] ^ T) | (ga[3] ^ T) | (gh[1] ^ T)) == 0u)) break;
      __builtin_amdgcn_s_sleep(HSLEEP);
    }
    STAMP(11);
    const float hm = (ne & 1) ? hi16(gh[0]) : lo16(gh[0]);
    const float c0 = __int_as_float(__builtin_amdgcn_readfirstlane((int)ga[0]));
    const float ea = lane < 40 ? __uint_as_float(ga[0]) - c0 : 0.f;
    float u = swap32_sum(ea, lane < 40 ? fmaf(16.f * ea, ea, __uint_as_float(ga[2])) : 0.f);
    u = row_sum(u); u = swap16_sum(u, u);
    const float dm = rdl(u, 0) * (1.f / 40.f);
    const float mean = c0 + dm, var = fmaxf(fmaf(-dm, dm, rdl(u, 32) * (1.f / DM)), 0.f);
    const float mb = rbf(mean), inv = __builtin_amdgcn_rcpf(rbf(__builtin_amdgcn_sqrtf(rbf(rbf(var) + 1e-5f))));
    const float mb2 = rbf((mean - mb) * inv), inv2 = __builtin_amdgcn_rcpf(rbf(__builtin_amdgcn_sqrtf(rbf(rbf(var * inv * inv) + 1e-5f))));
    const float cur = rbf(rbf(rbf(rbf(hm - mb) * inv) - mb2) * inv2);
    if (l5 < 3) { L.t[2 * l5 + hf] = cur; L.hv[2 * l5 + hf] = hm; }
    if (bid == 0 && lane == 0) __hip_atomic_store((GAS unsigned*)(ws + WS_EPOCH), T, RLX_AGENT);
  }
  float fw1[3], fb1[3], fw2[3];
  {
    const bool odd = tid & 1;
    fw1[0] = odd ? hi16(mw1[0]) : lo16(mw1[0]); fw1[1] = odd ? lo16(mw1[1]) : hi16(mw1[0]); fw1[2] = odd ? hi16(mw1[1]) : lo16(mw1[1]);
    fb1[0] = odd ? hi16(mb1[0]) : lo16(mb1[0]); fb1[1] = odd ? lo16(mb1[1]) : hi16(mb1[0]); fb1[2] = odd ? hi16(mb1[1]) : lo16(mb1[1]);
    fw2[0] = odd ? hi16(mw2[0]) : lo16(mw2[0]); fw2[1] = odd ? lo16(mw2[1]) : hi16(mw2[0]); fw2[2] = odd ? hi16(mw2[1]) : lo16(mw2[1]);
    asm volatile("" : "+v"(fw1[0]), "+v"(fw1[1]), "+v"(fw1[2]), "+v"(fb1[0]), "+v"(fb1[1]), "+v"(fb1[2]), "+v"(fw2[0]), "+v"(fw2[1]), "+v"(fw2[2]));
  }
  BAR();
  STAMP(12);
  {
    constexpr float GA = -0.7978845608028654f * 2.f * LOG2E, GB = GA * 0.044715f;
    const v4f t03 = *(const v4f*)&L.t[0]; const v2f t45 = *(const v2f*)&L.t[4];
    v2f tp[3] = {v2f{t03[0], t03[1]}, v2f{t03[2], t03[3]}, t45}, a[3];
#pragma unroll
    for (int e = 0; e < 3; ++e) {
      v2f nu[3], de[3];
#pragma unroll
      for (int i = 0; i < 3; ++i) {
        const v2f u = pkfma(tp[e], v2f{fw1[i], fw1[i]}, v2f{fb1[i], fb1[i]});
        const v2f z = u * pkfma(u * u, v2f{GB, GB}, v2f{GA, GA});
        v2f ex;
        ex[0] = __builtin_amdgcn_exp2f(fminf(z[0], 40.f));
        ex[1] = e < 2 ? __builtin_amdgcn_exp2f(fminf(z[1], 40.f)) : 0.f;
        de[i] = ex + v2f{1.f, 1.f};
        nu[i] = u * v2f{fw2[i], fw2[i]};
      }
      const v2f d01 = de[0] * de[1];
      const v2f num = pkfma(pkfma(nu[0], de[1], nu[1] * de[0]), de[2], nu[2] * d01), den = d01 * de[2];
      a[e][0] = num[0] * __builtin_amdgcn_rcpf(den[0]);
      a[e][1] = e < 2 ? num[1] * __builtin_amdgcn_rcpf(den[1]) : 0.f;
    }
    float s = swap16_sum(swap32_sum(a[0][0], a[0][1]), swap32_sum(a[1][0], a[1][1]));
    float s4 = swap32_sum(a[2][0], a[2][0]); s4 = swap16_sum(s4, s4);
    s = row_sum(s); s4 = row_sum(s4);
    if ((lane & 15) == 0) L.red[(lane >> 5) + ((lane >> 3) & 2)][w] = s;
    if (lane == 1) L.red[4][w] = s4;
    BAR();
    if (tid < 5) {
      const v4f ra = *(const v4f*)&L.red[tid][0], rb = *(const v4f*)&L.red[tid][4];
      float y = ((ra[0] + ra[1]) + (ra[2] + ra[3])) + ((rb[0] + rb[1]) + (rb[2] + rb[3]));
      y = rbf(rbf(y) + bf2f(xres));
      out[b3 * DM + n0 + tid] = (u16)f2bf(L.hv[tid] + y);
    }
  }
  STAMP(13);
}
}

extern "C" void kernel_launch(void* const* d_in, const int* in_sizes, int n_in, void* d_out, int out_size, void* d_ws, size_t ws_size,
                              hipStream_t stream) {
  using namespace fk;
  hipLaunchKernelGGL(fused, dim3(256), dim3(512), 0, stream, (const u16*)d_in[0], (const char*)d_in[1], (const char*)d_in[2], (const char*)d_in[3],
                     (const u16*)d_in[4], (const u16*)d_in[6], (const u16*)d_in[7], (const u16*)d_in[8], (const u16*)d_in[9], (u16*)d_out, (char*)d_ws);
}
```

```cpp
#include <hip/hip_runtime.h>
#include <stdint.h>

namespace fk {
typedef unsigned short u16;
typedef unsigned long long u64;
typedef unsigned v2u __attribute__((ext_vector_type(2)));
typedef unsigned v3u __attribute__((ext_vector_type(3)));
typedef unsigned v4u __attribute__((ext_vector_type(4)));
typedef int v4i __attribute__((ext_vector_type(4)));
typedef float v4f __attribute__((ext_vector_type(4)));
typedef float v2f __attribute__((ext_vector_type(2)));
typedef short v4s __attribute__((ext_vector_type(4)));
typedef __bf16 bf16x2 __attribute__((ext_vector_type(2)));
typedef __bf16 bf16x8 __attribute__((ext_vector_type(8)));
#define GAS __attribute__((address_space(1)))
#define LAS __attribute__((address_space(3)))
#define RLX_AGENT __ATOMIC_RELAXED, __HIP_MEMORY_SCOPE_AGENT
#define NOUNROLL _Pragma("clang loop unroll(disable)")

constexpr int DM = 640, NH = 32, SK = 8192, DH = 192;
constexpr unsigned ROWB = 384, REGION = SK * ROWB, KVBYTES = 64u * REGION;
constexpr unsigned OOB = 0x40000000u;
constexpr int SC1 = 16, SC1NT = 18;
constexpr unsigned WS_EPOCH = 0;
constexpr unsigned WS_PROBE = 4096;
constexpr unsigned WS_LAG = 8192;
constexpr unsigned WS_Q = 12288;
constexpr unsigned WS_H = WS_Q + 2 * 2 * 6144 * 8;
constexpr unsigned WS_Y = WS_H + 2 * 40 * 128;
constexpr unsigned PREC = 896;
constexpr unsigned WS_P = WS_Y + 32 * 640 * 8;
constexpr unsigned WS_S = WS_P + 32 * 2 * 8 * PREC;
constexpr unsigned WS_END = WS_S + 64 * 8 * 8 * 384;
constexpr unsigned TAG_STEP = 0x9E3779B9u;
constexpr unsigned SPIN_CAP = 1u << 20;
constexpr float LOG2E = 1.4426950408889634f;
constexpr unsigned PACE = 107, PACE1 = 3;
constexpr int PACE_T0 = 630, STAG = 110;
constexpr unsigned XSLACK = 40;
constexpr unsigned NFIRST = 2;

__device__ __forceinline__ float bf2f(unsigned v) { return __uint_as_float(v << 16); }
__device__ __forceinline__ unsigned f2bf(float f) { return (unsigned)__builtin_bit_cast(u16, (__bf16)f); }
__device__ __forceinline__ float rbf(float f) { return bf2f(f2bf(f)); }
__device__ __forceinline__ float lo16(unsigned v) { return __uint_as_float(v << 16); }
__device__ __forceinline__ float hi16(unsigned v) { return __uint_as_float(v & 0xffff0000u); }
__device__ __forceinline__ v2f unpk(unsigned v) { return v2f{lo16(v), hi16(v)}; }
__device__ __forceinline__ v2f pkfma(v2f a, v2f b, v2f c) { return __builtin_elementwise_fma(a, b, c); }
__device__ __forceinline__ float dot2(unsigned a, unsigned b, float c) {
  return __builtin_amdgcn_fdot2_f32_bf16(__builtin_bit_cast(bf16x2, a), __builtin_bit_cast(bf16x2, b), c, false);
}
template <int CTRL> __device__ __forceinline__ float dpp(float x) {
  return __builtin_bit_cast(float, __builtin_amdgcn_mov_dpp(__builtin_bit_cast(int, x), CTRL, 0xf, 0xf, true));
}
constexpr int XOR1 = 0xB1, XOR2 = 0x4E, HMIR = 0x141, MIR = 0x140, ROR8 = 0x128;
__device__ __forceinline__ float row_sum(float x) {
  x += dpp<XOR1>(x); x += dpp<XOR2>(x); x += dpp<HMIR>(x); x += dpp<MIR>(x); return x;
}
__device__ __forceinline__ float row_max(float x) {
  x = fmaxf(x, dpp<XOR1>(x)); x = fmaxf(x, dpp<XOR2>(x)); x = fmaxf(x, dpp<HMIR>(x)); x = fmaxf(x, dpp<MIR>(x)); return x;
}
__device__ __forceinline__ float swap16_sum(float a, float b) {
  auto s = __builtin_amdgcn_permlane16_swap(__float_as_uint(a), __float_as_uint(b), false, false);
  return __uint_as_float(s[0]) + __uint_as_float(s[1]);
}
__device__ __forceinline__ float swap32_sum(float a, float b) {
  auto s = __builtin_amdgcn_permlane32_swap(__float_as_uint(a), __float_as_uint(b), false, false);
  return __uint_as_float(s[0]) + __uint_as_float(s[1]);
}
__device__ __forceinline__ float wave_sum(float x) { x = row_sum(x); x = swap16_sum(x, x); return swap32_sum(x, x); }
__device__ __forceinline__ float wave_max(float x) {
  x = row_max(x);
  auto s = __builtin_amdgcn_permlane16_swap(__float_as_uint(x), __float_as_uint(x), false, false);
  x = fmaxf(__uint_as_float(s[0]), __uint_as_float(s[1]));
  auto t = __builtin_amdgcn_permlane32_swap(__float_as_uint(x), __float_as_uint(x), false, false);
  return fmaxf(__uint_as_float(t[0]), __uint_as_float(t[1]));
}
__device__ __forceinline__ float rdl(float x, int l) { return __int_as_float(__builtin_amdgcn_readlane(__float_as_int(x), l)); }
__device__ __forceinline__ v4i mk_rsrc(const void* p, unsigned bytes) {
  const u64 a = (u64)p;
  v4i r; r[0] = (int)(unsigned)a; r[1] = (int)((a >> 32) & 0xffff); r[2] = (int)bytes; r[3] = 0x00020000; return r;
}
__device__ __forceinline__ void st_gran(char* ws, unsigned off, unsigned val, unsigned tag) {
  __hip_atomic_store((GAS u64*)(ws + off), ((u64)tag << 32) | val, RLX_AGENT);
}
__device__ __forceinline__ v2u pack24(float a, float b, unsigned tag16) {
  const unsigned x = __float_as_uint(a) + 0x80u, y = __float_as_uint(b) + 0x80u;
  return v2u{(x & 0xffffff00u) | ((y >> 8) & 0xffu), (y & 0xffff0000u) | tag16};
}
__device__ __forceinline__ float un24a(unsigned d0) { return __uint_as_float(d0 & 0xffffff00u); }
__device__ __forceinline__ float un24b(unsigned d0, unsigned d1) { return __uint_as_float(__builtin_amdgcn_perm(d1, d0, 0x0706000cu)); }
__device__ __forceinline__ unsigned tag24(const v4u& g) { return __builtin_amdgcn_perm(g[3], g[1], 0x05040100u); }
#define BAR() do { asm volatile("s_waitcnt lgkmcnt(0)" ::: "memory"); __builtin_amdgcn_s_barrier(); asm volatile("" ::: "memory"); } while (0)
__device__ __forceinline__ unsigned fcode(unsigned off) { return (((off >> 19) & 1u) << 2) ^ ((off >> 20) & 3u); }

template <bool B> struct BoolC { static constexpr bool value = B; };

struct __attribute__((aligned(16))) Lds {
  float xn[DM][2];
  float qp[8][128];
  unsigned q[64][4];
  float p[2][8][2][96];
  unsigned sc[2][8 * 72];
  float ow[2][8][200];
  char pad[12 * 1024];
  float ml[2][2];
  u16 ob[2][192];
  float t[8];
  float hv[8];
  float red[8][8];
  unsigned flag, labs, flag_pad[2];
  unsigned lag[64];
  u16 wt[192 * 80];
};

#define LOADBLK(R, rs, so, so5) asm volatile("s_nop 4\n\t" \
    "buffer_load_dwordx3 %0, %6, %8, %9 offen nt\n\t" \
    "buffer_load_dwordx3 %1, %6, %8, %9 offen offset:768 nt\n\t" \
    "buffer_load_dwordx3 %2, %6, %8, %9 offen offset:1536 nt\n\t" \
    "buffer_load_dwordx3 %3, %6, %8, %9 offen offset:2304 nt\n\t" \
    "buffer_load_dwordx3 %4, %6, %8, %9 offen offset:3072 nt\n\t" \
    "buffer_load_dwordx3 %5, %7, %8, %10 offen offset:3840 nt" \
    : "=&v"(R[0]), "=&v"(R[1]), "=&v"(R[2]), "=&v"(R[3]), "=&v"(R[4]), "=&v"(R[5]) \
    : "v"(voff), "v"(voff5), "s"(rs), "s"(so), "s"(so5) : "memory")
#define WAITV(n, r) asm volatile("s_waitcnt vmcnt(%1)" : "+v"(r) : "i"(n) : "memory")

__global__ void __launch_bounds__(576, 3)
fused(const u16* __restrict__ x, const char* __restrict__ K, const char* __restrict__ V, const char* __restrict__ wq, const u16* __restrict__ wo,
      const u16* __restrict__ w1, const u16* __restrict__ b1, const u16* __restrict__ w2, const u16* __restrict__ b2, u16* __restrict__ out, char* ws) {
  __shared__ Lds L;
  const int tid = (int)threadIdx.x - 64, lane = threadIdx.x & 63, w = __builtin_amdgcn_readfirstlane(threadIdx.x >> 6) - 1;
  const int bid = blockIdx.x, j = bid & 7, r = bid >> 3;
  const int hf = lane >> 5, l5 = lane & 31;
  const unsigned tstart = (unsigned)__builtin_amdgcn_s_memrealtime();
  unsigned tbase = tstart + PACE_T0 + (unsigned)((r - 16) * STAG / 32);
  auto until = [&](unsigned due) { NOUNROLL for (int sp = 0; sp < 4096 && (int)((unsigned)__builtin_amdgcn_s_memrealtime() - due) < 0; ++sp) __builtin_amdgcn_s_sleep(1); };
  unsigned rnd = (unsigned)(__builtin_amdgcn_s_memtime() >> 2);
  unsigned E;
  asm volatile("s_load_dword %0, %6, 0x0" : "=s"(E), "+s"(rnd), "+s"(x), "+s"(wq), "+s"(K), "+s"(V) : "s"(ws) : "memory");
#define GET_TAG(t) unsigned t; asm volatile("s_waitcnt lgkmcnt(0)\n\ts_add_u32 %0, %1, %2" : "=s"(t) : "s"(E), "i"((int)TAG_STEP) : "memory", "scc")
  const unsigned xcc = (unsigned)__builtin_amdgcn_s_getreg(20 | (3 << 11)) & 7u;
  const __amdgpu_buffer_rsrc_t rwb = __builtin_amdgcn_make_buffer_rsrc((void*)ws, 0, WS_END, 0x00020000);
  const v4i rw = mk_rsrc(ws, WS_END);
  const int h2 = 4 * j + (r >> 3), c2 = r & 7;
  const int b3 = j >> 2, n0 = ((j & 3) * 32 + r) * 5;

  auto mk_lab = [&](unsigned id) { return (((id >> 1) ^ (unsigned)(r >> 1)) & 1u) | ((id >> 2) << 1) | (((id ^ (unsigned)r) & 1u) << 2); };
  const unsigned labF = mk_lab((unsigned)j) ^ ((unsigned)(r >> 1) & 2u);

  auto publish = [&](unsigned b, unsigned lab, unsigned T) {
    const float* op = &L.ow[b][0][4 * min(lane, 47)];
    v4f v = *(const v4f*)op;
#pragma unroll
    for (int i = 1; i < 8; ++i) v += *(const v4f*)(op + 200 * i);
    const v2f ml = *(const v2f*)L.ml[b];
    const v2u g0 = pack24(v[0], v[1], T & 0xffffu), g1 = pack24(v[2], v[3], T >> 16);
    const v4u g = lane < 48 ? v4u{g0[0], g0[1], g1[0], g1[1]} : v4u{__float_as_uint(ml[0]), T, __float_as_uint(ml[1]), T};
    __builtin_amdgcn_raw_buffer_store_b128(g, rwb, lane <= 48 ? lane * 16 : OOB, WS_P + (((unsigned)r * 2u + b) * 8u + lab) * PREC, SC1);
  };
  auto p_ml = [&](unsigned b) { return __builtin_amdgcn_raw_buffer_load_b128(rwb, (lane & 7) * PREC + 768, WS_P + ((unsigned)h2 * 2u + b) * (8u * PREC), SC1); };
  auto p_get = [&](unsigned b, v4u (&g)[8]) {
#pragma unroll
    for (int s = 0; s < 8; ++s) g[s] = __builtin_amdgcn_raw_buffer_load_b128(rwb, min(lane, 47) * 16, WS_P + (((unsigned)h2 * 2u + b) * 8u + s) * PREC, SC1);
  };
  auto p_weights = [&](const v4u& ml) {
    const float ms = __uint_as_float(ml[0]);
    float M = fmaxf(ms, dpp<XOR1>(ms)); M = fmaxf(M, dpp<XOR2>(M)); M = fmaxf(M, dpp<HMIR>(M));
    const float wt = __builtin_amdgcn_exp2f((ms - M) * LOG2E);
    float Ls = wt * __uint_as_float(ml[2]); Ls += dpp<XOR1>(Ls); Ls += dpp<XOR2>(Ls); Ls += dpp<HMIR>(Ls);
    return wt * __builtin_amdgcn_rcpf(Ls);
  };
  auto p_bad = [&](const v4u (&g)[8], unsigned T) {
    unsigned bad = 0;
#pragma unroll
    for (int s = 0; s < 8; ++s) bad |= tag24(g[s]) ^ T;
    return bad;
  };
  auto p_sum = [&](unsigned b, const v4u (&g)[8], float wn) {
    v2f o0 = {0.f, 0.f}, o1 = {0.f, 0.f};
#pragma unroll
    for (int s = 0; s < 8; ++s) {
      const float c = rdl(wn, s);
      o0 = pkfma(v2f{c, c}, v2f{un24a(g[s][0]), un24b(g[s][0], g[s][1])}, o0); o1 = pkfma(v2f{c, c}, v2f{un24a(g[s][2]), un24b(g[s][2], g[s][3])}, o1);
    }
    *(v2u*)&L.ob[b][4 * min(lane, 47)] = v2u{f2bf(o0[0]) | (f2bf(o0[1]) << 16), f2bf(o1[0]) | (f2bf(o1[1]) << 16)};
  };
  volatile LAS unsigned* const vcnt = (volatile LAS unsigned*)&L.flag;
  volatile LAS unsigned* const vlabs = (volatile LAS unsigned*)&L.labs;

  if (__builtin_expect(w < 0, 1)) {
    __builtin_amdgcn_s_setprio(3);
    u64 t1, t2, t3;
    {
      const int rg = xcc & 3;
      const unsigned off = (unsigned)((rg & 1) * 32 + r) * REGION + (32u + (rnd & 31u)) * 32768u;
      const unsigned pa = fcode(off);
      const char* base = ((rg & 2) ? V : K) + off + ((rnd >> 5) & 31u) * 128u;
      const char* A = base + pa * 4096;
      const char* B = base + (pa ^ 2u) * 4096;
      unsigned va, vb; const unsigned z = 0;
      asm volatile("global_load_dword %3, %5, %6 nt\n\t"
                   "global_load_dword %4, %5, %7 nt\n\t"
                   "s_waitcnt vmcnt(1)\n\ts_memtime %0\n\t"
                   "s_waitcnt vmcnt(0)\n\ts_memtime %1\n\ts_memrealtime %2\n\t"
                   "s_waitcnt lgkmcnt(0)" : "=&s"(t1), "=&s"(t2), "=&s"(t3), "=&v"(va), "=&v"(vb) : "v"(z), "s"(A), "s"(B) : "memory");
    }
    GET_TAG(T);
    if (lane == 0) { st_gran(ws, WS_PROBE + r * 128 + j * 8, ((unsigned)t3 << 16) | (min((unsigned)(t2 - t1), 200u) << 8) | xcc, T); *vcnt = 0u; *vlabs = ~0u; }
  }
  int role = w;
  asm volatile("" : "+s"(role));
  if (role < 0) {
    GET_TAG(T);
    unsigned nextpub = 0, seen = 0;
    int shift = 0;
    BAR();
    const unsigned vp = WS_PROBE + (lane & 7) * 8 + (unsigned)((lane >> 4) == 1 ? max(r - 1, 0) : ((lane >> 4) == 2 ? min(r + 1, 31) : r)) * 128u;
    until(tstart + 200u);
    v2u pg;
    asm volatile("buffer_load_dwordx2 %0, %1, %2, 0 offen sc1" : "=&v"(pg) : "v"(vp), "s"(rw) : "memory");
    NOUNROLL
    for (unsigned spin = 0; __builtin_amdgcn_readfirstlane(*vcnt) != 8 && spin < SPIN_CAP; ++spin) __builtin_amdgcn_s_sleep(1);
    {
      asm volatile("" ::: "memory");
      v2f a = *(const v2f*)&L.qp[0][2 * lane];
#pragma unroll
      for (int i = 1; i < 8; ++i) a += *(const v2f*)&L.qp[i][2 * lane];
      __builtin_amdgcn_raw_buffer_store_b128(v4u{__float_as_uint(a[0]), T, __float_as_uint(a[1]), T}, rwb, hf * (6144 * 8) + l5 * 16,
                                             r < 24 ? WS_Q + (r & 1) * (2 * 6144 * 8) + (12 * j + (r >> 1)) * 512 : OOB, SC1);
    }
    WAITV(1, pg);
    NOUNROLL
    for (unsigned spin = 0; !__all(pg[1] == T) && spin < SPIN_CAP; ++spin)
      asm volatile("buffer_load_dwordx2 %0, %1, %2, 0 offen sc1\n\ts_waitcnt vmcnt(0)" : "=&v"(pg) : "v"(vp), "s"(rw) : "memory");
    unsigned labs = labF * 0x01010101u;
    {
      const int gx = (int)pg[0] & 7;
      int msk = 1 << gx;
      msk |= __builtin_amdgcn_mov_dpp(msk, XOR1, 0xf, 0xf, true); msk |= __builtin_amdgcn_mov_dpp(msk, XOR2, 0xf, 0xf, true); msk |= __builtin_amdgcn_mov_dpp(msk, HMIR, 0xf, 0xf, true);
      const int gv = (__builtin_amdgcn_ds_permute(((lane & 48) + gx) * 4, (int)pg[0]) >> 8) & 255;
      float dg = (float)(__builtin_amdgcn_mov_dpp(gv, 0x104, 0xf, 0xf, true) - gv);
      if ((lane >> 4) == 3) dg *= 0.25f;
      dg = swap16_sum(dg, dg); dg = swap32_sum(dg, dg);
      const unsigned far = (unsigned)__builtin_amdgcn_ballot_w64(dg > 0.f) & 15u;
      if (__all(msk == 0xff))
        labs = (mk_lab(xcc) * 0x01010101u) ^ 0x04000400u ^ ((far & 1u) << 1) ^ ((far & 2u) << 8) ^ ((far & 4u) << 15) ^ ((far & 8u) << 22);
    }
    if (lane == 0) *vlabs = labs;
    {
      const float dt = fminf(fmaxf((float)(short)((pg[0] >> 16) - tstart), -200.f), 600.f);
      tbase += (unsigned)((int)(rdl(wave_sum(dt), 0) * (1.f / 64.f)) - 100);
    }
    until(tstart + 400u);
    NOUNROLL
    for (unsigned spin = 0; spin < SPIN_CAP; ++spin) {
      asm volatile("" ::: "memory");
      v4u g[6]; unsigned bad = 0;
#pragma unroll
      for (int i = 0; i < 6; ++i) g[i] = __builtin_amdgcn_raw_buffer_load_b128(rwb, hf * (6144 * 8) + l5 * 48 + (i % 3) * 16, WS_Q + (i / 3) * (2 * 6144 * 8) + r * (DH * 8), SC1);
#pragma unroll
      for (int i = 0; i < 6; ++i) bad |= (g[i][1] ^ T) | (g[i][3] ^ T);
#pragma unroll
      for (int i = 0; i < 3; ++i) L.q[lane][i] = f2bf(__uint_as_float(g[i][0]) + __uint_as_float(g[i + 3][0])) | (f2bf(__uint_as_float(g[i][2]) + __uint_as_float(g[i + 3][2])) << 16);
      if (__all(bad == 0u)) break;
    }
    until(tbase);
    BAR();
    L.lag[lane] = ~T;
    unsigned n = 0;
    auto ticks = [&](unsigned cnt) {
      NOUNROLL
      for (unsigned i = 0; i < cnt; ++i, ++n) {
        tbase += PACE + (((n + 5u) % 24u >= 12u) ? PACE1 : 0u);
        until(tbase);
        BAR();
        if (n >= 12u && n < 41u) {
          const int late = (int)((unsigned)__builtin_amdgcn_s_memrealtime() - tbase);
          if (late > 80) {
            if (n >= nextpub) { if (lane == 0) st_gran(ws, WS_LAG + r * 128 + j * 8, (unsigned)min(shift + late, 4000) | (n << 16), T); nextpub = n + 2u; }
          } else {
            const u64 g = *(volatile LAS u64*)&L.lag[2 * (lane & 7)];
            if (lane < 16) {
              unsigned keep;
              asm volatile("s_mov_b32 %0, m0\n\ts_mov_b32 m0, %2\n\ts_nop 0\n\tglobal_load_lds_dword %1, off sc1\n\ts_mov_b32 m0, %0"
                           : "=&s"(keep) : "v"(ws + WS_LAG + r * 128 + lane * 4), "s"((unsigned)(size_t)(LAS unsigned*)L.lag) : "memory");
            }
            const unsigned gn = (unsigned)g >> 16;
            const bool fresh = (unsigned)(g >> 32) == T && gn > seen;
            seen = fresh ? gn : seen;
            float e = fresh ? fmaxf((float)((int)((unsigned)g & 0xffffu) - shift), 0.f) : 0.f;
            e += dpp<XOR1>(e); e += dpp<XOR2>(e); e += dpp<HMIR>(e);
            const int d = (int)(rdl(e, 0) * 0.2f);
            shift += d; tbase += (unsigned)d;
          }
        }
      }
    };
    {
      const unsigned vs[3] = {lane < 48 ? WS_P + ((unsigned)r * 16u + ((labs >> 16) & 7u)) * PREC + lane * 16u : OOB,
                              lane < 48 ? WS_P + ((unsigned)r * 16u + 8u + ((labs >> 24) & 7u)) * PREC + lane * 16u : OOB,
                              lane < 40 ? WS_Y + (unsigned)(h2 * DM + 80 * c2) * 8u + lane * 16u : OOB};
      ticks(4u);
      v4u old[3];
#pragma unroll
      for (int k = 0; k < 3; ++k) old[k] = __builtin_amdgcn_raw_buffer_load_b128(rwb, vs[k], 0, SC1NT);
      ticks(3u);
#pragma unroll
      for (int k = 0; k < 3; ++k) {
        const unsigned d = tag24(old[k]) ^ T;
        if ((d & 0xffffu) == 0u || (d >> 16) == 0u) __builtin_amdgcn_raw_buffer_store_b128(v4u{0u, ~T, 0u, ~T >> 16}, rwb, vs[k], 0, SC1);
      }
      ticks(6u);
    }
    NOUNROLL
    for (unsigned b = 0; b < 2u; ++b) {
      const unsigned lv = (labs >> (16u + 8u * b)) & 7u;
      unsigned vi[3], e[3][3];
#pragma unroll
      for (int k = 0; k < 3; ++k) {
        const unsigned un = lane + 64 * k, idx = un - 24u * ((un * 171u) >> 12);
        const unsigned lk = (b == 0u && idx < 2u * NFIRST) ? labF : (labs >> (8u * b)) & 7u;
        const unsigned rec = WS_S + (b * 32u + (unsigned)r) * (8u * 3072u) + un * 16u;
#pragma unroll
        for (int i = 0; i < 3; ++i) e[k][i] = L.sc[b][3 * un + i];
        vi[k] = lk != lv ? rec + lv * 3072u : OOB;
        __builtin_amdgcn_raw_buffer_store_b128(v4u{e[k][0], (e[k][1] & 0xffffu) | (T << 16), e[k][2], (e[k][1] >> 16) | (T & 0xffff0000u)}, rwb, lk != lv ? rec + lk * 3072u : OOB, 0, SC1NT);
      }
      if (((labs >> (8u * b)) & 7u) != lv) tbase += XSLACK;
      ticks(6u);
      v4u sg[3];
#pragma unroll
      for (int k = 0; k < 3; ++k) sg[k] = __builtin_amdgcn_raw_buffer_load_b128(rwb, vi[k], 0, SC1NT);
      ticks(2u);
      NOUNROLL
      for (unsigned spin = 0; spin < SPIN_CAP; ++spin) {
        bool ok = true;
#pragma unroll
        for (int k = 0; k < 3; ++k) ok &= vi[k] == OOB || ((sg[k][1] >> 16) | (sg[k][3] & 0xffff0000u)) == T;
        if (__all(ok)) break;
        __builtin_amdgcn_s_sleep(2);
        asm volatile("" ::: "memory");
#pragma unroll
        for (int k = 0; k < 3; ++k) sg[k] = __builtin_amdgcn_raw_buffer_load_b128(rwb, vi[k], 0, SC1NT);
      }
      float mx = -INFINITY;
#pragma unroll
      for (int k = 0; k < 3; ++k) {
        const bool in = vi[k] != OOB;
        e[k][0] = in ? sg[k][0] : e[k][0]; e[k][1] = in ? (sg[k][1] & 0xffffu) | (sg[k][3] << 16) : e[k][1]; e[k][2] = in ? sg[k][2] : e[k][2];
#pragma unroll
        for (int i = 0; i < 3; ++i) mx = fmaxf(mx, fmaxf(lo16(e[k][i]), hi16(e[k][i])));
      }
      const float M = wave_max(mx);
      float ls = 0.f;
#pragma unroll
      for (int k = 0; k < 3; ++k) {
        const unsigned un = lane + 64 * k, wv = (un * 171u) >> 12, idx = un - 24u * wv;
        v4f pa, pb;
#pragma unroll
        for (int i = 0; i < 3; ++i) { pa[i] = __builtin_amdgcn_exp2f((lo16(e[k][i]) - M) * LOG2E); pb[i] = __builtin_amdgcn_exp2f((hi16(e[k][i]) - M) * LOG2E); ls += pa[i] + pb[i]; }
        pa[3] = 0.f; pb[3] = 0.f;
        *(v4f*)&L.p[b][wv][0][4 * idx] = pa; *(v4f*)&L.p[b][wv][1][4 * idx] = pb;
      }
      ls = wave_sum(ls);
      if (lane == 0) *(v2f*)L.ml[b] = v2f{M, ls};
      ticks(4u);
    }
    publish(0u, (labs >> 16) & 7u, T);
    ticks(4u);
    v4u g[8], ml = p_ml(0u);
    p_get(0u, g);
    ticks(2u);
    NOUNROLL
    for (unsigned spin = 0; !__all((p_bad(g, T) | (ml[1] ^ T) | (ml[3] ^ T)) == 0u) && spin < SPIN_CAP; ++spin) {
      __builtin_amdgcn_s_sleep(2);
      asm volatile("" ::: "memory");
      ml = p_ml(0u); p_get(0u, g);
    }
    p_sum(0u, g, p_weights(ml));
    asm volatile("s_waitcnt vmcnt(0) lgkmcnt(0)" ::: "memory");
    return;
  }

  v4u xs4 = {0u, 0u, 0u, 0u}; unsigned xs1 = 0;
  __builtin_amdgcn_s_sleep(4);
  if (w < 2) {
    const u16* xp = x + w * DM + lane * 10;
    asm volatile("global_load_dwordx4 %0, %2, off\n\tglobal_load_dword %1, %2, off offset:16" : "=&v"(xs4), "=&v"(xs1) : "v"(xp) : "memory");
  }
  v4u wqv[5];
  {
    const v4i rq = mk_rsrc(wq, DM * NH * DH * 2);
    const unsigned so = (r < 24 ? 0u : OOB) + (unsigned)(320 * (r & 1) + 40 * w) * (NH * DH * 2) + (12 * j + (r >> 1)) * 128;
    const unsigned vq = (lane >> 3) * (NH * DH * 2) + (lane & 7) * 16;
#pragma unroll
    for (int i = 0; i < 5; ++i) { const unsigned si = so + i * 8 * (NH * DH * 2); asm volatile("buffer_load_dwordx4 %0, %1, %2, %3 offen nt" : "=&v"(wqv[i]) : "v"(vq), "s"(rq), "s"(si) : "memory"); }
  }
  __builtin_amdgcn_sched_barrier(0);
  auto mk_tab = [&](unsigned lab4, unsigned first) {
    const unsigned ph = ((unsigned)lane * 43u) >> 9;
    const unsigned st = (unsigned)lane - 12u * ph, per = ((r & 1) ? 11u - st : st) * 8u + (unsigned)w;
    const unsigned rbase = ((ph & 1u) * 32u + (unsigned)r) * REGION;
    const unsigned k = per * 8u + (((unsigned)lane < NFIRST ? first : (lab4 >> ((8u * ph) & 31u)) & 7u) ^ fcode(rbase + per * 32768u));
    const unsigned r0 = ((32u * k + 1u) * 43691u) >> 17, r1 = ((32u * k + 33u) * 43691u) >> 17;
    return lane < 48 ? rbase + r0 * ROWB + (r1 - r0 - 10u) : OOB;
  };
  unsigned tabv = mk_tab(0u, labF);
  const v4i rk = mk_rsrc(K, KVBYTES), rv = mk_rsrc(V, KVBYTES);
  const unsigned voff = lane * 12, voff5 = hf ? OOB : voff;
  v3u ring[4][6];
  auto issue = [&](int d, unsigned rev, const v4i& rs) {
    const unsigned v = (unsigned)__builtin_amdgcn_readlane((int)tabv, (int)(rev * 4u) + d);
    const unsigned so = v & ~1u, so5 = (v & 1u) ? so : OOB;
    LOADBLK(ring[d], rs, so, so5);
  };
  GET_TAG(T);
  asm volatile("s_waitcnt vmcnt(5)" : "+v"(xs4), "+v"(xs1) :: "memory");
  if (w < 2) {
    float e[10];
#pragma unroll
    for (int i = 0; i < 4; ++i) { e[2 * i] = lo16(xs4[i]); e[2 * i + 1] = hi16(xs4[i]); }
    e[8] = lo16(xs1); e[9] = hi16(xs1);
    const float c0 = __int_as_float(__builtin_amdgcn_readfirstlane(__float_as_int(e[0])));
    float s = 0.f, v = 0.f;
#pragma unroll
    for (int i = 0; i < 10; ++i) { const float d = e[i] - c0; s += d; v = fmaf(d, d, v); }
    float u = swap32_sum(s, v); u = row_sum(u); u = swap16_sum(u, u);
    const float dm = rdl(u, 0) * (1.f / DM), var = fmaxf(fmaf(-dm, dm, rdl(u, 32) * (1.f / DM)), 0.f);
    const float mb = rbf(c0 + dm), inv = __builtin_amdgcn_rcpf(rbf(__builtin_amdgcn_sqrtf(rbf(rbf(var) + 1e-5f))));
#pragma unroll
    for (int i = 0; i < 10; ++i) L.xn[lane * 10 + i][w] = rbf(rbf(e[i] - mb) * inv);
  }
  until(tstart + 100u);
  issue(0, 0u, rk);
  BAR();
  {
    v2f acc[2][4];
#pragma unroll
    for (int c = 0; c < 4; ++c) { acc[0][c] = v2f{0.f, 0.f}; acc[1][c] = v2f{0.f, 0.f}; }
#pragma unroll
    for (int i = 0; i < 5; ++i) {
      const v2f xv = *(const v2f*)L.xn[320 * (r & 1) + 40 * w + 8 * i + (lane >> 3)];
      WAITV(10 - i, wqv[i]);
#pragma unroll
      for (int c = 0; c < 4; ++c) {
        const v2f a = unpk(wqv[i][c]);
        acc[0][c] = pkfma(a, v2f{xv[0], xv[0]}, acc[0][c]);
        acc[1][c] = pkfma(a, v2f{xv[1], xv[1]}, acc[1][c]);
      }
    }
    float s1[8], s2[4];
#pragma unroll
    for (int c = 0; c < 8; ++c) s1[c] = swap32_sum(acc[0][c >> 1][c & 1], acc[1][c >> 1][c & 1]);
#pragma unroll
    for (int c = 0; c < 4; ++c) { float t = swap16_sum(s1[c], s1[c + 4]); t += dpp<ROR8>(t); s2[c] = t; }
    if ((lane & 8) == 0) *(v4f*)&L.qp[w][hf * 64 + 8 * (lane & 7) + 4 * ((lane >> 4) & 1)] = v4f{s2[0], s2[1], s2[2], s2[3]};
    asm volatile("" ::: "memory");
    if (lane == 0) __hip_atomic_fetch_add((LAS unsigned*)&L.flag, 1u, __ATOMIC_RELAXED, __HIP_MEMORY_SCOPE_WORKGROUP);
  }
  const int g3 = (j & 3) * 10 + r;
  v2u mw1, mb1, mw2; unsigned xres;
  {
    const unsigned vm = (unsigned)tid * 6u & ~3u;
    const u16* xr = w == 7 ? x + b3 * DM + min(g3, 39) * 16 + 2 * (lane & 7) + hf : b2;
    asm volatile("global_load_dwordx2 %0, %4, %5\n\tglobal_load_dwordx2 %1, %4, %6\n\tglobal_load_dwordx2 %2, %4, %7\n\tglobal_load_ushort %3, %8, off"
                 : "=&v"(mw1), "=&v"(mb1), "=&v"(mw2), "=&v"(xres) : "v"(vm), "s"(w1), "s"(b1), "s"(w2), "v"(xr) : "memory");
  }
  until(tstart + 240u);
  issue(1, 0u, rk);
  unsigned labs = ~0u;
  NOUNROLL
  for (unsigned spin = 0; labs == ~0u && spin < 8u * SPIN_CAP; ++spin) { labs = __builtin_amdgcn_readfirstlane(*vlabs); __builtin_amdgcn_s_sleep(1); }
  tabv = mk_tab(labs, labF);
  const u64 n11 = __builtin_amdgcn_ballot_w64((tabv & 1u) != 0u);
  until(tstart + 346u);
  issue(2, 0u, rk);
  until(tstart + 453u);
  issue(3, 0u, rk);
  asm volatile("s_waitcnt vmcnt(18)" : "+v"(mw1), "+v"(mb1), "+v"(mw2), "+v"(xres) :: "memory");
  BAR();

  const bool slot_ok = (l5 & 15) < 12;
  const int slot = (l5 >> 4) * 12 + (l5 & 15);
  NOUNROLL
  for (unsigned rev = 0; rev < 6; ++rev) {
    const unsigned ph = rev >= 3u;
    const v3u qv = *(const v3u*)L.q[ph * 32 + l5];
    const v4i rs = rev == 5u ? rv : rk;
    const unsigned e4 = (unsigned)(n11 >> (rev * 4u));
    float col[2] = {0.f, 0.f};
#pragma unroll
    for (int d = 0; d < 4; ++d) {
#pragma unroll
      for (int u = 0; u < 6; ++u) {
        WAITV(23 - u, ring[d][u]);
        float s = dot2(ring[d][u][0], qv[0], 0.f);
        s = dot2(ring[d][u][1], qv[1], s);
        s = dot2(ring[d][u][2], qv[2], s);
        s = row_sum(s);
        if (u == 5) s = (hf == 0 && ((e4 >> d) & 1u)) ? s : -INFINITY;
        col[d >> 1] = ((lane & 15) == (d & 1) * 6 + u) ? s : col[d >> 1];
      }
      asm volatile("" : "+v"(col[d >> 1]));
      issue(d, rev + 1u, rs);
      BAR();
    }
    const float sc = rbf(swap16_sum(col[0], col[1])) * 0.07216878364870322f;
    if (slot_ok) ((u16*)&L.sc[0][w * 72 + rev * 24u + ph * (8 * 72 - 72) + slot])[hf] = (u16)f2bf(sc);
  }

  v2f acc[3] = {v2f{0.f, 0.f}, v2f{0.f, 0.f}, v2f{0.f, 0.f}};
  v4u wov[4];
  auto load_wo = [&]() {
    unsigned vo[4];
#pragma unroll
    for (int i = 0; i < 4; ++i) { const unsigned c = tid + 512 * i, row = (c * 6554u) >> 16; vo[i] = c * 16u + row * (DM * 2 - 160); }
    if (tid >= 384) vo[3] = OOB;
    const v4i ro = mk_rsrc(wo, DM * NH * DH * 2);
    const unsigned so = (unsigned)(h2 * DH * DM + 80 * c2) * 2;
    asm volatile("buffer_load_dwordx4 %0, %4, %8, %9 offen nt\n\tbuffer_load_dwordx4 %1, %5, %8, %9 offen nt\n\t"
                 "buffer_load_dwordx4 %2, %6, %8, %9 offen nt\n\tbuffer_load_dwordx4 %3, %7, %8, %9 offen nt"
                 : "=&v"(wov[0]), "=&v"(wov[1]), "=&v"(wov[2]), "=&v"(wov[3])
                 : "v"(vo[0]), "v"(vo[1]), "v"(vo[2]), "v"(vo[3]), "s"(ro), "s"(so) : "memory");
  };
  auto vrev = [&](unsigned rev, auto last) {
    const float* pp = &L.p[0][w][hf][rev * 32u + (rev >= 3u ? 8 * 2 * 96 - 96 : 0)];
    v4f pr[4][2];
#pragma unroll
    for (int d = 0; d < 4; ++d) { pr[d][0] = *(const v4f*)(pp + d * 8); pr[d][1] = *(const v4f*)(pp + d * 8 + 4); }
#pragma unroll
    for (int d = 0; d < 4; ++d) {
#pragma unroll
      for (int u = 0; u < 6; ++u) {
        WAITV((last.value ? 23 - 6 * d + (d > 2 ? 4 : 0) : 23) - u, ring[d][u]);
        const float p = pr[d][u / 3][u % 3];
#pragma unroll
        for (int i = 0; i < 3; ++i) acc[i] = pkfma(unpk(ring[d][u][i]), v2f{p, p}, acc[i]);
        if (last.value) asm volatile("" : "+v"(acc[0]), "+v"(acc[1]), "+v"(acc[2]));
      }
      if (!last.value) {
        asm volatile("" : "+v"(acc[0]), "+v"(acc[1]), "+v"(acc[2]));
        issue(d, 7u + rev, rv);
        if (d < 3 || rev < 4u) BAR();
      } else if (d == 2) {
        load_wo();
      }
    }
  };
  NOUNROLL
  for (unsigned rev = 0; rev < 5; ++rev) {
    vrev(rev, BoolC<false>{});
    if (rev == 2u) {
#pragma unroll
      for (int i = 0; i < 3; ++i) { L.ow[0][w][6 * l5 + 2 * i + hf] = swap32_sum(acc[i][0], acc[i][1]); acc[i] = v2f{0.f, 0.f}; }
    }
  }
  vrev(5u, BoolC<true>{});
#pragma unroll
  for (int i = 0; i < 3; ++i) L.ow[1][w][6 * l5 + 2 * i + hf] = swap32_sum(acc[i][0], acc[i][1]);
  BAR();
  if (w == 0) publish(1u, labs >> 24, T);
  v4u pg[8], pml;
  if (w == 5) { pml = p_ml(1u); p_get(1u, pg); }
  asm volatile("s_waitcnt vmcnt(1)\n\ts_cmp_eq_u32 %4, 0\n\ts_cbranch_scc1 1f\n\ts_waitcnt vmcnt(0)\n1:" : "+v"(wov[0]), "+v"(wov[1]), "+v"(wov[2]), "+v"(wov[3]) : "s"(w) : "memory", "scc");
#pragma unroll
  for (int i = 0; i < 4; ++i) if (i < 3 || tid < 384) *(v4u*)&L.wt[(tid + 512 * i) * 8] = wov[i];
  BAR();

  v4s wof[6][2];
  if (w < 5) {
    const LAS u16* bp = (const LAS u16*)L.wt + (8 * (lane >> 4) + ((lane & 15) >> 2)) * 80 + 16 * w + 4 * (lane & 3);
#pragma unroll
    for (int ks = 0; ks < 6; ++ks)
#pragma unroll
      for (int t = 0; t < 2; ++t) wof[ks][t] = __builtin_amdgcn_ds_read_tr16_b64_v4i16((LAS v4s*)(bp + (32 * ks + 4 * t) * 80));
  } else if (w == 5) {
    float wn = p_weights(pml);
    NOUNROLL
    for (unsigned spin = 0; !__all((p_bad(pg, T) | (pml[1] ^ T) | (pml[3] ^ T)) == 0u) && spin < SPIN_CAP; ++spin) {
      asm volatile("" ::: "memory");
      pml = p_ml(1u);
      p_get(1u, pg);
      __builtin_amdgcn_sched_barrier(0);
      wn = p_weights(pml);
      __builtin_amdgcn_sched_barrier(0);
    }
    p_sum(1u, pg, wn);
  }
  BAR();
  if (w < 5) {
    v4f c = {0.f, 0.f, 0.f, 0.f};
#pragma unroll
    for (int ks = 0; ks < 6; ++ks) {
      const v4u a = *(const v4u*)&L.ob[lane & 1][32 * ks + 8 * (lane >> 4)];
      c = __builtin_amdgcn_mfma_f32_16x16x32_bf16(__builtin_bit_cast(bf16x8, a), __builtin_bit_cast(bf16x8, __builtin_shufflevector(wof[ks][0], wof[ks][1], 0, 1, 2, 3, 4, 5, 6, 7)), c, 0, 0, 0);
    }
    __builtin_amdgcn_raw_buffer_store_b64(pack24(c[0], c[1], (lane & 1) ? T >> 16 : T & 0xffffu), rwb, lane < 16 ? lane * 8 : OOB, WS_Y + (unsigned)(h2 * DM + 80 * c2 + 16 * w) * 8u, SC1);
  }
  v2f zc[4], nc[2]; float zd[4], nd[2];
  {
    constexpr float GA = -0.7978845608028654f * 2.f * LOG2E, GB = GA * 0.044715f;
    const bool odd = tid & 1;
    const v2f a = {odd ? hi16(mw1[0]) : lo16(mw1[0]), odd ? lo16(mw1[1]) : hi16(mw1[0])}, c = {odd ? hi16(mb1[0]) : lo16(mb1[0]), odd ? lo16(mb1[1]) : hi16(mb1[0])};
    const v2f e = {odd ? hi16(mw2[0]) : lo16(mw2[0]), odd ? lo16(mw2[1]) : hi16(mw2[0])};
    const float a2 = odd ? hi16(mw1[1]) : lo16(mw1[1]), c2v = odd ? hi16(mb1[1]) : lo16(mb1[1]), e2 = odd ? hi16(mw2[1]) : lo16(mw2[1]);
    zc[3] = GB * a * a * a; zc[2] = 3.f * GB * a * a * c; zc[1] = a * (3.f * GB * c * c + GA); zc[0] = c * (GB * c * c + GA); nc[1] = a * e; nc[0] = c * e;
    zd[3] = GB * a2 * a2 * a2; zd[2] = 3.f * GB * a2 * a2 * c2v; zd[1] = a2 * (3.f * GB * c2v * c2v + GA); zd[0] = c2v * (GB * c2v * c2v + GA); nd[1] = a2 * e2; nd[0] = c2v * e2;
    asm volatile("" : "+v"(zc[0]), "+v"(zc[1]), "+v"(zc[2]), "+v"(zc[3]), "+v"(nc[0]), "+v"(nc[1]), "+v"(zd[0]), "+v"(zd[1]), "+v"(zd[2]), "+v"(zd[3]), "+v"(nd[0]), "+v"(nd[1]));
  }

  if (w == 7) {
    if (r < 10) {
      v4u y[4];
      NOUNROLL
      for (unsigned spin = 0; spin < SPIN_CAP; ++spin) {
        asm volatile("" ::: "memory");
        unsigned bad = 0;
#pragma unroll
        for (int i = 0; i < 4; ++i) y[i] = __builtin_amdgcn_raw_buffer_load_b128(rwb, (lane >> 3) * (DM * 8) + (lane & 7) * 16, WS_Y + 8 * i * (DM * 8) + g3 * 128, SC1);
#pragma unroll
        for (int i = 0; i < 4; ++i) bad |= tag24(y[i]) ^ T;
        if (__all(bad == 0u)) break;
        __builtin_amdgcn_s_sleep(1);
      }
      const unsigned sel = b3 ? 0x0706000cu : 0x0302010cu;
      float e0 = 0.f, e1 = 0.f;
#pragma unroll
      for (int i = 0; i < 4; ++i) { e0 += __uint_as_float(__builtin_amdgcn_perm(y[i][1], y[i][0], sel)); e1 += __uint_as_float(__builtin_amdgcn_perm(y[i][3], y[i][2], sel)); }
      float a = swap32_sum(e0, e1); a = swap16_sum(a, a); a += dpp<ROR8>(a);
      const unsigned hb = f2bf(bf2f(xres) + rbf(a));
      const float hv = bf2f(hb);
      const float c0 = rdl(hv, 0), dv = hv - c0;
      float u = swap32_sum(dv, dv * dv); u += dpp<XOR1>(u); u += dpp<XOR2>(u); u += dpp<HMIR>(u);
      const float dm = rdl(u, 0) * (1.f / 16.f), m = c0 + dm, q = fmaxf(fmaf(-16.f * dm, dm, rdl(u, 32)), 0.f);
      auto sw = __builtin_amdgcn_permlane32_swap(hb, hb, false, false);
      const unsigned val = lane == 6 ? __float_as_uint(m) : (lane == 7 ? __float_as_uint(q) : sw[0] | (sw[1] << 16));
      __builtin_amdgcn_raw_buffer_store_b64(v2u{val, T}, rwb, lane >= 6 && lane < 16 ? (lane - 6) * 8 : OOB, WS_H + (b3 * 40 + g3) * 128, SC1);
    }
    v4u ga; v2u gh;
    const int ne = n0 + min(2 * l5 + hf, 4);
    NOUNROLL
    for (unsigned spin = 0; spin < SPIN_CAP; ++spin) {
      asm volatile("" ::: "memory");
      ga = __builtin_amdgcn_raw_buffer_load_b128(rwb, min(lane, 39) * 128, WS_H + b3 * (40 * 128), SC1);
      gh = __builtin_amdgcn_raw_buffer_load_b64(rwb, (ne >> 4) * 128 + 16 + ((ne & 15) >> 1) * 8, WS_H + b3 * (40 * 128), SC1);
      if (__all(((ga[1] ^ T) | (ga[3] ^ T) | (gh[1] ^ T)) == 0u)) break;
      __builtin_amdgcn_s_sleep(1);
    }
    const float hm = (ne & 1) ? hi16(gh[0]) : lo16(gh[0]);
    const float c0 = __int_as_float(__builtin_amdgcn_readfirstlane((int)ga[0]));
    const float ea = lane < 40 ? __uint_as_float(ga[0]) - c0 : 0.f;
    float u = swap32_sum(ea, lane < 40 ? fmaf(16.f * ea, ea, __uint_as_float(ga[2])) : 0.f);
    u = row_sum(u); u = swap16_sum(u, u);
    const float dm = rdl(u, 0) * (1.f / 40.f);
    const float mean = c0 + dm, var = fmaxf(fmaf(-dm, dm, rdl(u, 32) * (1.f / DM)), 0.f);
    const float mb = rbf(mean), inv = __builtin_amdgcn_rcpf(rbf(__builtin_amdgcn_sqrtf(rbf(rbf(var) + 1e-5f))));
    const float mb2 = rbf((mean - mb) * inv), inv2 = __builtin_amdgcn_rcpf(rbf(__builtin_amdgcn_sqrtf(rbf(rbf(var * inv * inv) + 1e-5f))));
    const float cur = rbf(rbf(rbf(rbf(hm - mb) * inv) - mb2) * inv2);
    if (l5 < 3) { L.t[2 * l5 + hf] = cur; L.hv[2 * l5 + hf] = hm; }
    if (bid == 0 && lane == 0) __hip_atomic_store((GAS unsigned*)(ws + WS_EPOCH), T, RLX_AGENT);
  }
  BAR();
  {
    const v4f t03 = *(const v4f*)&L.t[0]; const float t4 = L.t[4];
    const v2f tp[2] = {v2f{t03[0], t03[1]}, v2f{t03[2], t03[3]}};
    v2f a[3];
    auto term = [&](v2f t, v2f c3, v2f c2, v2f c1, v2f c0, v2f n1, v2f n0, v2f acc) {
      const v2f z = pkfma(pkfma(pkfma(c3, t, c2), t, c1), t, c0);
      const v2f rc = {__builtin_amdgcn_rcpf(1.f + __builtin_amdgcn_exp2f(z[0])), __builtin_amdgcn_rcpf(1.f + __builtin_amdgcn_exp2f(z[1]))};
      return pkfma(pkfma(n1, t, n0), rc, acc);
    };
#define BC(x) v2f{x, x}
#pragma unroll
    for (int e = 0; e < 2; ++e) {
      a[e] = v2f{0.f, 0.f};
#pragma unroll
      for (int i = 0; i < 2; ++i) a[e] = term(tp[e], BC(zc[3][i]), BC(zc[2][i]), BC(zc[1][i]), BC(zc[0][i]), BC(nc[1][i]), BC(nc[0][i]), a[e]);
      a[e] = term(tp[e], BC(zd[3]), BC(zd[2]), BC(zd[1]), BC(zd[0]), BC(nd[1]), BC(nd[0]), a[e]);
    }
    a[2] = term(BC(t4), zc[3], zc[2], zc[1], zc[0], nc[1], nc[0], v2f{0.f, 0.f});
    a[2][0] = fmaf(fmaf(nd[1], t4, nd[0]), __builtin_amdgcn_rcpf(1.f + __builtin_amdgcn_exp2f(fmaf(fmaf(fmaf(zd[3], t4, zd[2]), t4, zd[1]), t4, zd[0]))), a[2][0] + a[2][1]);
    float s = swap16_sum(swap32_sum(a[0][0], a[0][1]), swap32_sum(a[1][0], a[1][1]));
    float s4 = swap32_sum(a[2][0], a[2][0]); s4 = swap16_sum(s4, s4);
    s = row_sum(s); s4 = row_sum(s4);
    if ((lane & 15) == 0) L.red[(lane >> 5) + ((lane >> 3) & 2)][w] = s;
    if (lane == 1) L.red[4][w] = s4;
    BAR();
    if (tid < 5) {
      const v4f ra = *(const v4f*)&L.red[tid][0], rb = *(const v4f*)&L.red[tid][4];
      float y = ((ra[0] + ra[1]) + (ra[2] + ra[3])) + ((rb[0] + rb[1]) + (rb[2] + rb[3]));
      y = rbf(rbf(y) + bf2f(xres));
      out[b3 * DM + n0 + tid] = (u16)f2bf(L.hv[tid] + y);
    }
  }
}
}

extern "C" void kernel_launch(void* const* d_in, const int* in_sizes, int n_in, void* d_out, int out_size, void* d_ws, size_t ws_size,
                              hipStream_t stream) {
  using namespace fk;
  hipLaunchKernelGGL(fused, dim3(256), dim3(576), 0, stream, (const u16*)d_in[0], (const char*)d_in[1], (const char*)d_in[2], (const char*)d_in[3],
                     (const u16*)d_in[4], (const u16*)d_in[6], (const u16*)d_in[7], (const u16*)d_in[8], (const u16*)d_in[9], (u16*)d_out, (char*)d_ws);
}
```
